```python
import torch

def _softmax(x, axis=-1):
    return torch.softmax(x, dim=axis)
def _gelu(x):
    return torch.nn.functional.gelu(x, approximate="tanh")
def _silu(x):
    return torch.nn.functional.silu(x)
def _sigmoid(x):
    return torch.sigmoid(x)
def _layernorm(x, axis=-1, eps=1e-5):
    m = x.mean(dim=axis, keepdim=True)
    v = x.var(dim=axis, keepdim=True, unbiased=False)
    return (x - m) / torch.sqrt(v + eps)
def _scatter_add(tgt, idx, src, axis=0):
    return tgt.clone().index_add_(axis, idx, src)
def _segment_sum(src, seg, n):
    out = torch.zeros((n,) + src.shape[1:], dtype=src.dtype)
    return out.index_add_(0, seg, src)
def _pad(x, pw):
    flat = tuple(v for pair in reversed(pw) for v in pair)
    return torch.nn.functional.pad(x, flat)

kernel_name = 'standalone__low_rank_proj_linformer__prompt'

def setup_inputs():

    _w = (lambda a: (lambda t: t.to(torch.bfloat16) if t.is_floating_point() else t)(torch.as_tensor(a)))
    B, H, Sq, Skv, Dh, R = 1, 32, 2048, 2048, 192, 8
    return {
        "q": _w((lambda *s: torch.randn(*s, dtype=torch.bfloat16) * 0.8)(B, H, Sq, Dh)),
        "k": _w((lambda *s: torch.randn(*s, dtype=torch.bfloat16) * 0.8)(B, H, Skv, Dh)),
        "v": _w((lambda *s: torch.randn(*s, dtype=torch.bfloat16) * 0.05)(B, H, Skv, Dh)),
        "e_proj": _w((lambda *s: torch.randn(*s, dtype=torch.bfloat16) * 0.8)(R, Skv) * (Skv ** -0.5)),
        "f_proj": _w((lambda *s: torch.randn(*s, dtype=torch.bfloat16) * 0.05)(R, Skv) * (Skv ** -0.5)),
    }

def reference(q, k, v, e_proj, f_proj):
    B, H, Sq, Dh = q.shape
    kp = torch.einsum("rn,bhnd->bhrd", e_proj, k)
    vp = torch.einsum("rn,bhnd->bhrd", f_proj, v)
    scores = torch.einsum("bhqd,bhrd->bhqr", q, kp) * (Dh ** -0.5)
    p = _softmax(scores, axis=-1)
    o = torch.einsum("bhqr,bhrd->bhqd", p, vp)
    return o
```

```cpp
#include <hip/hip_runtime.h>
#include <hip/hip_bf16.h>
#include <stdint.h>

#define GAS __attribute__((address_space(1)))
#define LAS __attribute__((address_space(3)))
typedef unsigned int u32;
typedef u32 v4u __attribute__((ext_vector_type(4)));
typedef u32 v2u __attribute__((ext_vector_type(2)));
typedef float f32x4 __attribute__((ext_vector_type(4)));
typedef short s16x8 __attribute__((ext_vector_type(8)));
typedef short s16x4 __attribute__((ext_vector_type(4)));

constexpr int H = 32, S = 2048, D = 192, R = 8;
constexpr int ROWB = D * 2;
constexpr int NWG = 256;
constexpr int PART_F4 = 384;

#define LD_NT 1
#define ST_NT 0

__device__ __forceinline__ u32 as_u(float x) { return __builtin_bit_cast(u32, x); }
__device__ __forceinline__ float as_f(u32 x) { return __builtin_bit_cast(float, x); }
__device__ __forceinline__ f32x4 mfma32(v4u a, v4u b, f32x4 c) {
  return __builtin_amdgcn_mfma_f32_16x16x32_bf16(__builtin_bit_cast(s16x8, a), __builtin_bit_cast(s16x8, b), c, 0, 0, 0);
}
__device__ __forceinline__ f32x4 mfma16(v2u a, v2u b, f32x4 c) {
  return __builtin_amdgcn_mfma_f32_16x16x16bf16_1k(__builtin_bit_cast(s16x4, a), __builtin_bit_cast(s16x4, b), c, 0, 0, 0);
}
__device__ __forceinline__ v4u ldx(const GAS v4u* p) {
  return __builtin_nontemporal_load(p);
}
__device__ __forceinline__ void stx(GAS v4u* p, v4u v) {
  *p = v;
}
typedef __bf16 bf16x2 __attribute__((ext_vector_type(2)));
typedef float f32x2 __attribute__((ext_vector_type(2)));
__device__ __forceinline__ u32 cvt_pk(float lo, float hi) {
  f32x2 v = {lo, hi};
  bf16x2 b = __builtin_convertvector(v, bf16x2);
  return __builtin_bit_cast(u32, b);
}
__device__ __forceinline__ float ror8(float x) {
  return __builtin_bit_cast(float, __builtin_amdgcn_update_dpp(0, __builtin_bit_cast(int, x), 0x128, 0xf, 0xf, true));
}
__device__ __forceinline__ float ror8_hi(float old, float x) {
  return __builtin_bit_cast(float, __builtin_amdgcn_update_dpp(__builtin_bit_cast(int, old), __builtin_bit_cast(int, x), 0x128, 0xf, 0xc, false));
}

__device__ __forceinline__ void proj_pack(const f32x4 (&acc)[8], f32x4 (&z)[2]) {
#pragma unroll
  for (int e = 0; e < 2; ++e) {
#pragma unroll
    for (int reg = 0; reg < 4; ++reg) {
      auto s0 = __builtin_amdgcn_permlane32_swap(as_u(acc[4 * e + 0][reg]), as_u(acc[4 * e + 1][reg]), false, false);
      auto s1 = __builtin_amdgcn_permlane32_swap(as_u(acc[4 * e + 2][reg]), as_u(acc[4 * e + 3][reg]), false, false);
      float x = as_f(s0[0]) + ror8(as_f(s0[1]));
      float y = as_f(s1[0]) + ror8(as_f(s1[1]));
      z[e][reg] = ror8_hi(x, y);
    }
  }
}

__global__ void __launch_bounds__(512, 2) k_proj(const uint16_t* __restrict__ Kp, const uint16_t* __restrict__ Vp,
                                                 const uint16_t* __restrict__ Ep, const uint16_t* __restrict__ Fp,
                                                 float* __restrict__ part) {
  extern __shared__ __attribute__((aligned(16))) unsigned char smem[];
  LAS f32x4* red = (LAS f32x4*)smem;
  const int b = blockIdx.x;
  const int h = (b & 7) * 4 + (b >> 6), j = (b >> 3) & 7;
  const int t = j & 1, c = j >> 1;
  const int tid = threadIdx.x, lane = tid & 63, w = __builtin_amdgcn_readfirstlane(tid >> 6);
  const uint16_t* X = t ? Vp : Kp;
  const uint16_t* P = t ? Fp : Ep;
  const int n0 = 512 * c + 64 * w;
  const int p = (lane >> 3) & 1, g4 = lane >> 4, ch = lane & 7;
  const v4u ef = *(const GAS v4u*)(P + (size_t)(lane & 7) * S + n0 + 16 * g4 + 8 * p);
  const GAS unsigned char* xb = (const GAS unsigned char*)X + ((size_t)h * S + n0 + 16 * g4 + 8 * p) * ROWB + 16 * ch;
  v4u x[3][8];
#pragma unroll
  for (int s = 0; s < 3; ++s)
#pragma unroll
    for (int jj = 0; jj < 8; ++jj) x[s][jj] = ldx((const GAS v4u*)(xb + jj * ROWB + 128 * s));
  __builtin_amdgcn_sched_barrier(0);
#pragma unroll
  for (int s = 0; s < 3; ++s) {
    f32x4 acc[8];
#pragma unroll
    for (int dd = 0; dd < 8; ++dd) {
      v4u f;
#pragma unroll
      for (int i = 0; i < 4; ++i)
        f[i] = __builtin_amdgcn_perm(x[s][2 * i + 1][dd >> 1], x[s][2 * i][dd >> 1], (dd & 1) ? 0x07060302u : 0x05040100u);
      acc[dd] = mfma32(ef, f, f32x4{0.f, 0.f, 0.f, 0.f});
    }
    f32x4 z[2];
    proj_pack(acc, z);
    red[(w * 6 + s * 2 + 0) * 64 + lane] = z[0];
    red[(w * 6 + s * 2 + 1) * 64 + lane] = z[1];
  }
  __syncthreads();
  if (tid < PART_F4) {
    f32x4 v = red[tid];
#pragma unroll
    for (int ww = 1; ww < 8; ++ww) v += red[ww * PART_F4 + tid];
    ((GAS f32x4*)part)[(size_t)(h * 8 + j) * PART_F4 + tid] = v;
  }
}

constexpr int Q_OFF = 0;
constexpr int KP_OFF = 98304;
constexpr int KP_STRIDE = 400;
constexpr int VT_OFF = KP_OFF + 8 * KP_STRIDE;
constexpr int LDS2 = VT_OFF + 192 * 16;

__device__ __forceinline__ void glds16(const GAS void* g, u32 lds_base) {
  u32 keep;
  asm volatile("s_mov_b32 %0, m0\n\ts_mov_b32 m0, %2\n\ts_nop 0\n\tglobal_load_lds_dwordx4 %1, off nt\n\ts_mov_b32 m0, %0" : "=&s"(keep) : "v"(g), "s"(lds_base) : "memory");
}

__global__ void __launch_bounds__(512, 2) k_attn(const uint16_t* __restrict__ Qp, const float* __restrict__ part, uint16_t* __restrict__ Op) {
  extern __shared__ __attribute__((aligned(16))) unsigned char smem[];
  LAS unsigned char* L = (LAS unsigned char*)smem;
  const int b = blockIdx.x;
  const int h = (b & 7) * 4 + (b >> 6), j = (b >> 3) & 7;
  const int tid = threadIdx.x, lane = tid & 63, w = __builtin_amdgcn_readfirstlane(tid >> 6);
  const size_t tile = ((size_t)h * S + 256 * j + 32 * w) * ROWB;
  const u32 lbase = (u32)(size_t)smem + Q_OFF + w * 12288;
  {
    const GAS unsigned char* qb = (const GAS unsigned char*)Qp + tile;
    const int g = lane >> 4, bb = lane & 15;
#pragma unroll
    for (int i = 0; i < 12; ++i) {
      const int G = 4 * i + g, rho = (G * 43) >> 7;
      glds16(qb + 256 * G + 16 * (bb ^ (rho & 15)), lbase + i * 1024);
    }
  }
  if (tid < PART_F4) {
    const GAS f32x4* pp = (const GAS f32x4*)part + (size_t)h * 8 * PART_F4 + tid;
    f32x4 a[8];
#pragma unroll
    for (int q = 0; q < 8; ++q) a[q] = pp[q * PART_F4];
    const f32x4 kv = (a[0] + a[2]) + (a[4] + a[6]);
    const f32x4 vv = (a[1] + a[3]) + (a[5] + a[7]);
    const int se = tid >> 6, s = se >> 1, e = se & 1;
    const int ch = lane & 7, be = (lane >> 3) & 1, ga = (lane >> 4) & 1, et = lane >> 5;
    const int d = 64 * s + 8 * ch + 4 * e + 2 * be + et;
    const u32 k01 = cvt_pk(kv[0], kv[1]), k23 = cvt_pk(kv[2], kv[3]);
    LAS uint16_t* kp = (LAS uint16_t*)(L + KP_OFF + (4 * ga) * KP_STRIDE + 2 * d);
    kp[0] = (uint16_t)k01;
    kp[KP_STRIDE / 2] = (uint16_t)(k01 >> 16);
    kp[KP_STRIDE] = (uint16_t)k23;
    kp[3 * KP_STRIDE / 2] = (uint16_t)(k23 >> 16);
    *(LAS v2u*)(L + VT_OFF + d * 16 + ga * 8) = v2u{cvt_pk(vv[0], vv[1]), cvt_pk(vv[2], vv[3])};
  }
  asm volatile("s_waitcnt vmcnt(0) lgkmcnt(0)" ::: "memory");
  __builtin_amdgcn_s_barrier();
  asm volatile("" ::: "memory");
  const int n = lane & 15, g = lane >> 4;
  v4u F[6];
#pragma unroll
  for (int s = 0; s < 6; ++s) F[s] = *(const LAS v4u*)(L + KP_OFF + (n & 7) * KP_STRIDE + 64 * s + 16 * g);
  f32x4 d0 = {0.f, 0.f, 0.f, 0.f}, d1 = {0.f, 0.f, 0.f, 0.f};
  const LAS unsigned char* qrow = L + Q_OFF + w * 12288 + n * 768;
#pragma unroll
  for (int s = 0; s < 6; ++s) {
    const int c0 = 4 * s + g, c1 = 24 + 4 * s + g;
    const v4u q0 = *(const LAS v4u*)(qrow + 16 * (c0 ^ n));
    const v4u q1 = *(const LAS v4u*)(qrow + 16 * (c1 ^ n));
    d0 = mfma32(F[s], q0, d0);
    d1 = mfma32(F[s], q1, d1);
  }
  v2u VB[3][8];
#pragma unroll
  for (int ga = 0; ga < 3; ++ga) {
    const int c = 16 * ga + n;
    const int pin = c >= 24 ? 1 : 0, cr = c - 24 * pin;
    const bool on = pin == (g >> 1);
#pragma unroll
    for (int i = 0; i < 8; ++i) {
      const v2u v = *(const LAS v2u*)(L + VT_OFF + (8 * cr + i) * 16 + (g & 1) * 8);
      VB[ga][i] = on ? v : v2u{0u, 0u};
    }
  }
  const float sc = 0.07216878364870322f * 1.4426950408889634f;
  float sv[4];
#pragma unroll
  for (int r = 0; r < 4; ++r) sv[r] = (lane < 32 ? d0[r] : d1[r]);
  float m = fmaxf(fmaxf(sv[0], sv[1]), fmaxf(sv[2], sv[3]));
  {
    auto t = __builtin_amdgcn_permlane16_swap(__builtin_bit_cast(u32, m), __builtin_bit_cast(u32, m), false, false);
    m = fmaxf(as_f(t[0]), as_f(t[1]));
  }
  float ps[4];
#pragma unroll
  for (int r = 0; r < 4; ++r) ps[r] = __builtin_amdgcn_exp2f((sv[r] - m) * sc);
  float sum = (ps[0] + ps[1]) + (ps[2] + ps[3]);
  {
    auto t = __builtin_amdgcn_permlane16_swap(__builtin_bit_cast(u32, sum), __builtin_bit_cast(u32, sum), false, false);
    sum = as_f(t[0]) + as_f(t[1]);
  }
  const float inv = 1.0f / sum;
  const v2u pa = v2u{cvt_pk(ps[0] * inv, ps[1] * inv), cvt_pk(ps[2] * inv, ps[3] * inv)};
  GAS unsigned char* ob = (GAS unsigned char*)Op + tile + (4 * g) * 768 + 16 * n;
#pragma unroll
  for (int ga = 0; ga < 3; ++ga) {
    f32x4 o[8];
#pragma unroll
    for (int i = 0; i < 8; ++i) o[i] = mfma16(pa, VB[ga][i], f32x4{0.f, 0.f, 0.f, 0.f});
#pragma unroll
    for (int reg = 0; reg < 4; ++reg) {
      v4u v = {cvt_pk(o[0][reg], o[1][reg]), cvt_pk(o[2][reg], o[3][reg]), cvt_pk(o[4][reg], o[5][reg]), cvt_pk(o[6][reg], o[7][reg])};
      stx((GAS v4u*)(ob + reg * 768 + 256 * ga), v);
    }
  }
}

extern "C" void kernel_launch(void* const* d_in, const int* in_sizes, int n_in, void* d_out, int out_size, void* d_ws, size_t ws_size,
                              hipStream_t stream) {
  const uint16_t* q = (const uint16_t*)d_in[0];
  const uint16_t* k = (const uint16_t*)d_in[1];
  const uint16_t* v = (const uint16_t*)d_in[2];
  const uint16_t* e = (const uint16_t*)d_in[3];
  const uint16_t* f = (const uint16_t*)d_in[4];
  float* part = (float*)d_ws;
  hipFuncSetAttribute((const void*)k_proj, hipFuncAttributeMaxDynamicSharedMemorySize, 49152);
  hipFuncSetAttribute((const void*)k_attn, hipFuncAttributeMaxDynamicSharedMemorySize, LDS2);
  k_proj<<<NWG, 512, 49152, stream>>>(k, v, e, f, part);
  k_attn<<<NWG, 512, LDS2, stream>>>(q, part, (uint16_t*)d_out);
}
```

```cpp
#include <hip/hip_runtime.h>
#include <hip/hip_bf16.h>
#include <stdint.h>

#define GAS __attribute__((address_space(1)))
#define LAS __attribute__((address_space(3)))
typedef unsigned int u32;
typedef unsigned long long u64;
typedef u32 v4u __attribute__((ext_vector_type(4)));
typedef u32 v2u __attribute__((ext_vector_type(2)));
typedef float f32x4 __attribute__((ext_vector_type(4)));
typedef float f32x2 __attribute__((ext_vector_type(2)));
typedef short s16x8 __attribute__((ext_vector_type(8)));
typedef short s16x4 __attribute__((ext_vector_type(4)));
typedef __bf16 bf16x2 __attribute__((ext_vector_type(2)));
typedef GAS u64 gu64;

constexpr int H = 32, S = 2048, D = 192, R = 8;
constexpr int ROWB = D * 2;
constexpr u32 TENS_BYTES = (u32)H * S * ROWB;
constexpr int NWG = 256;

#define NQ0 3
#define QWIN 6
#define POLL_SLEEP 1
#define PART_AUX 16
#define ST_AUX 18

constexpr size_t WS_EPOCH = 0;
constexpr size_t WS_FLAG = 4096;
constexpr size_t WS_PART = 16384;
constexpr size_t PART_HEAD = 12 * 2048;
constexpr size_t WS_PART_BYTES = H * PART_HEAD;
#define STAMP(i)

constexpr int Q_OFF = 0;
constexpr int RED_OFF = 98304;
constexpr int KP_OFF = RED_OFF + 32768;
constexpr int KP_STRIDE = 400;
constexpr int VT_OFF = KP_OFF + 8 * KP_STRIDE;
constexpr int CTL_OFF = VT_OFF + 192 * 16;
constexpr int LDS_BYTES = CTL_OFF + 64;

__device__ __forceinline__ u32 as_u(float x) { return __builtin_bit_cast(u32, x); }
__device__ __forceinline__ float as_f(u32 x) { return __builtin_bit_cast(float, x); }
__device__ __forceinline__ f32x4 mfma32(v4u a, v4u b, f32x4 c) {
  return __builtin_amdgcn_mfma_f32_16x16x32_bf16(__builtin_bit_cast(s16x8, a), __builtin_bit_cast(s16x8, b), c, 0, 0, 0);
}
__device__ __forceinline__ f32x4 mfma16(v2u a, v2u b, f32x4 c) {
  return __builtin_amdgcn_mfma_f32_16x16x16bf16_1k(__builtin_bit_cast(s16x4, a), __builtin_bit_cast(s16x4, b), c, 0, 0, 0);
}
__device__ __forceinline__ u32 cvt_pk(float lo, float hi) {
  f32x2 v = {lo, hi};
  bf16x2 b = __builtin_convertvector(v, bf16x2);
  return __builtin_bit_cast(u32, b);
}
__device__ __forceinline__ float ror8(float x) {
  return as_f((u32)__builtin_amdgcn_update_dpp(0, (int)as_u(x), 0x128, 0xf, 0xf, true));
}
__device__ __forceinline__ float ror8_hi(float old, float x) {
  return as_f((u32)__builtin_amdgcn_update_dpp((int)as_u(old), (int)as_u(x), 0x128, 0xf, 0xc, false));
}
__device__ __forceinline__ void proj_pack(const f32x4 (&acc)[8], f32x4 (&z)[2]) {
#pragma unroll
  for (int e = 0; e < 2; ++e) {
#pragma unroll
    for (int reg = 0; reg < 4; ++reg) {
      auto s0 = __builtin_amdgcn_permlane32_swap(as_u(acc[4 * e + 0][reg]), as_u(acc[4 * e + 1][reg]), false, false);
      auto s1 = __builtin_amdgcn_permlane32_swap(as_u(acc[4 * e + 2][reg]), as_u(acc[4 * e + 3][reg]), false, false);
      const float x = as_f(s0[0]) + ror8(as_f(s0[1]));
      const float y = as_f(s1[0]) + ror8(as_f(s1[1]));
      z[e][reg] = ror8_hi(x, y);
    }
  }
}
typedef __amdgpu_buffer_rsrc_t rsrc_t;
template <int IMM> __device__ __forceinline__ void ld_nt(v4u& d, u32 voff, rsrc_t rs, u32 soff) {
  asm volatile("buffer_load_dwordx4 %0, %1, %2, %3 offen offset:%4 nt" : "=v"(d) : "v"(voff), "s"(rs), "s"(soff), "i"(IMM));
}
__device__ __forceinline__ void ld_plain(v4u& d, u32 voff, rsrc_t rs, u32 soff) {
  asm volatile("buffer_load_dwordx4 %0, %1, %2, %3 offen" : "=v"(d) : "v"(voff), "s"(rs), "s"(soff));
}
__device__ __forceinline__ void dma_nt(u32 lds_dst, u32 voff, rsrc_t rs, u32 soff) {
  asm volatile("s_mov_b32 m0, %0\n\ts_nop 0\n\tbuffer_load_dwordx4 %1, %2, %3 offen nt lds" ::"s"(lds_dst), "v"(voff), "s"(rs), "s"(soff) : "memory");
}
__device__ __forceinline__ u32 q_piece_off(int i, int g, int bb) {
  const int G = 4 * i + g, rho = (G * 43) >> 7;
  return (u32)(256 * G + 16 * (bb ^ rho));
}
template <int N> __device__ __forceinline__ void wait_vm() { asm volatile("s_waitcnt vmcnt(%0)" ::"i"(N) : "memory"); }

template <int BASE, int I, int END> struct QPace {
  static __device__ __forceinline__ void run(u32 lb0, u32 lb1, u32 so0, u32 so1, rsrc_t rq, int g, int bb) {
    if constexpr (I < END) {
      if constexpr (I >= QWIN) wait_vm<QWIN - 1>();
      const int piece = NQ0 + I / 2;
      dma_nt(((I & 1) ? lb1 : lb0) + piece * 1024, q_piece_off(piece, g, bb), rq, (I & 1) ? so1 : so0);
      QPace<BASE, I + 1, END>::run(lb0, lb1, so0, so1, rq, g, bb);
    }
  }
};

__global__ void __launch_bounds__(512, 2)
linformer_fused(const uint16_t* __restrict__ Qp, const uint16_t* __restrict__ Kp, const uint16_t* __restrict__ Vp,
                const uint16_t* __restrict__ Ep, const uint16_t* __restrict__ Fp, uint16_t* __restrict__ Op, unsigned char* __restrict__ ws) {
  extern __shared__ __attribute__((aligned(16))) unsigned char smem[];
  LAS unsigned char* L = (LAS unsigned char*)smem;
  STAMP(0);
  const int b = blockIdx.x;
  const int h = (b & 7) * 4 + (b >> 6), j = (b >> 3) & 7;
  const int t = j >> 2, jj = j & 3;
  const int tid = threadIdx.x, lane = tid & 63, w = __builtin_amdgcn_readfirstlane(tid >> 6);
  const rsrc_t rx = __builtin_amdgcn_make_buffer_rsrc((void*)(t ? Vp : Kp), 0, TENS_BYTES, 0x00020000);
  const rsrc_t rp = __builtin_amdgcn_make_buffer_rsrc((void*)(t ? Fp : Ep), 0, R * S * 2, 0x00020000);
  const rsrc_t rq = __builtin_amdgcn_make_buffer_rsrc((void*)Qp, 0, TENS_BYTES, 0x00020000);
  const rsrc_t rw = __builtin_amdgcn_make_buffer_rsrc((void*)ws, 0, (u32)(WS_PART + WS_PART_BYTES), 0x00020000);
  const int g = lane >> 4, n = lane & 15;

  v2u ep;
  asm volatile("buffer_load_dwordx2 %0, off, %1, %2 sc1" : "=v"(ep) : "s"(rw), "s"((u32)(WS_EPOCH + h * 128)));
  const int u0 = 24 * jj + 3 * w;
  const u32 voffE = (u32)((lane & 7) * (S * 2) + (16 * g + 8 * ((lane >> 3) & 1)) * 2);
  const u32 voffX = (u32)((16 * g + 8 * ((lane >> 3) & 1)) * ROWB + 16 * (lane & 7));
  v4u ef[3], x[3][8];
#pragma unroll
  for (int i = 0; i < 3; ++i) ld_plain(ef[i], voffE, rp, (u32)(((u0 + i) & 31) * 128));
#pragma unroll
  for (int i = 0; i < 3; ++i) {
    const u32 so = (u32)((h * S + ((u0 + i) & 31) * 64) * ROWB + 128 * ((u0 + i) >> 5));
    ld_nt<0 * ROWB>(x[i][0], voffX, rx, so); ld_nt<1 * ROWB>(x[i][1], voffX, rx, so);
    ld_nt<2 * ROWB>(x[i][2], voffX, rx, so); ld_nt<3 * ROWB>(x[i][3], voffX, rx, so);
    ld_nt<4 * ROWB>(x[i][4], voffX, rx, so); ld_nt<5 * ROWB>(x[i][5], voffX, rx, so);
    ld_nt<6 * ROWB>(x[i][6], voffX, rx, so); ld_nt<7 * ROWB>(x[i][7], voffX, rx, so);
  }
  const u32 ldsb = (u32)(size_t)smem;
  const u32 qtile = (u32)((h * S + 256 * j + 32 * w) * ROWB);
#pragma unroll
  for (int i = 0; i < NQ0; ++i) dma_nt(ldsb + Q_OFF + w * 12288 + i * 1024, q_piece_off(i, g, n), rq, qtile);

  const int slo = (24 * jj) >> 5;
  const int sA = u0 >> 5, sB = (u0 + 2) >> 5;
  LAS f32x4* red = (LAS f32x4*)(L + RED_OFF);
  {
    const f32x4 z0 = {0.f, 0.f, 0.f, 0.f};
    if (sA != slo) { red[(0 * 8 + w) * 128 + lane] = z0; red[(0 * 8 + w) * 128 + 64 + lane] = z0; }
    if (sB == slo) { red[(1 * 8 + w) * 128 + lane] = z0; red[(1 * 8 + w) * 128 + 64 + lane] = z0; }
    if (tid == 0) *(LAS u32*)(L + CTL_OFF) = 0u;
  }

  f32x4 acc[8];
#define UNIT(i, ZERO)                                                                                                \
  _Pragma("unroll") for (int dd = 0; dd < 8; ++dd) {                                                                 \
    v4u f;                                                                                                           \
    _Pragma("unroll") for (int k = 0; k < 4; ++k)                                                                    \
        f[k] = __builtin_amdgcn_perm(x[i][2 * k + 1][dd >> 1], x[i][2 * k][dd >> 1], (dd & 1) ? 0x07060302u : 0x05040100u); \
    acc[dd] = mfma32(ef[i], f, ZERO ? f32x4{0.f, 0.f, 0.f, 0.f} : acc[dd]);                                          \
  }
#define FLUSH(strip)                                                                                                 \
  {                                                                                                                  \
    f32x4 z[2];                                                                                                      \
    proj_pack(acc, z);                                                                                               \
    const int sl = ((strip) != slo) ? 1 : 0;                                                                         \
    red[(sl * 8 + w) * 128 + lane] = z[0];                                                                           \
    red[(sl * 8 + w) * 128 + 64 + lane] = z[1];                                                                      \
  }
#define WAITX(i, N)                                                                                                  \
  asm volatile("s_waitcnt vmcnt(%8)"                                                                                 \
               : "+v"(x[i][0]), "+v"(x[i][1]), "+v"(x[i][2]), "+v"(x[i][3]), "+v"(x[i][4]), "+v"(x[i][5]), "+v"(x[i][6]), "+v"(x[i][7]) \
               : "i"(N))
  asm volatile("s_waitcnt vmcnt(%4)" : "+v"(ep), "+v"(ef[0]), "+v"(ef[1]), "+v"(ef[2]) : "i"(24 + NQ0));
  WAITX(0, 16 + NQ0);
  UNIT(0, true)
  const int s1 = (u0 + 1) >> 5;
  if (s1 != sA) {
    FLUSH(sA)
#pragma unroll
    for (int dd = 0; dd < 8; ++dd) acc[dd] = f32x4{0.f, 0.f, 0.f, 0.f};
  }
  WAITX(1, 8 + NQ0);
  UNIT(1, false)
  if (sB != s1) {
    FLUSH(s1)
#pragma unroll
    for (int dd = 0; dd < 8; ++dd) acc[dd] = f32x4{0.f, 0.f, 0.f, 0.f};
  }
  WAITX(2, NQ0);
  STAMP(1);
  UNIT(2, false)
  FLUSH(sB)
  asm volatile("s_waitcnt lgkmcnt(0)" ::: "memory");
  __builtin_amdgcn_s_barrier();
  asm volatile("" ::: "memory");
  STAMP(2);

  const u64 tag = (((u64)(u32)__builtin_amdgcn_readfirstlane(ep[1]) << 32) | (u32)__builtin_amdgcn_readfirstlane(ep[0])) + 1ull;
  gu64* flags = (gu64*)(ws + WS_FLAG + h * 128);
  volatile LAS u32* ctl = (volatile LAS u32*)(L + CTL_OFF);

  if (w >= 4) {
    const u32 lb0 = ldsb + Q_OFF + (w - 4) * 12288, lb1 = ldsb + Q_OFF + w * 12288;
    const u32 so0 = qtile - 4 * 32 * ROWB, so1 = qtile;
    QPace<0, 0, 2 * (12 - NQ0)>::run(lb0, lb1, so0, so1, rq, g, n);
    wait_vm<0>();
    STAMP(3);
  } else {
    const bool has_hi = (jj == 1 || jj == 2);
    if (w == 0 || (w == 1 && has_hi)) {
      f32x4 v0 = red[(w * 8 + 0) * 128 + lane], v1 = red[(w * 8 + 0) * 128 + 64 + lane];
#pragma unroll
      for (int ww = 1; ww < 8; ++ww) { v0 += red[(w * 8 + ww) * 128 + lane]; v1 += red[(w * 8 + ww) * 128 + 64 + lane]; }
      const int slot = 6 * t + 2 * jj - (jj > 0 ? 1 : 0) + w;
      const u32 po = (u32)(WS_PART + h * PART_HEAD + slot * 2048) + lane * 16;
      __builtin_amdgcn_raw_buffer_store_b128(__builtin_bit_cast(v4u, v0), rw, po, 0, PART_AUX);
      __builtin_amdgcn_raw_buffer_store_b128(__builtin_bit_cast(v4u, v1), rw, po + 1024, 0, PART_AUX);
      STAMP(3);
      asm volatile("s_waitcnt vmcnt(0)" ::: "memory");
      if (lane == 0) __hip_atomic_store(flags + slot, tag, __ATOMIC_RELAXED, __HIP_MEMORY_SCOPE_AGENT);
      STAMP(4);
    }
    if (w == 2) {
      u32 spins = 0;
      for (; spins < (1u << 22); ++spins) {
        const u64 f = __hip_atomic_load(flags + (lane < 12 ? lane : 0), __ATOMIC_RELAXED, __HIP_MEMORY_SCOPE_AGENT);
        if (__all(f == tag)) break;
        __builtin_amdgcn_s_sleep(POLL_SLEEP);
      }
      __builtin_amdgcn_fence(__ATOMIC_ACQUIRE, "wavefront");
      *ctl = 1u;
      if (j == 0 && lane == 0) __hip_atomic_store((gu64*)(ws + WS_EPOCH + h * 128), tag, __ATOMIC_RELAXED, __HIP_MEMORY_SCOPE_AGENT);
      STAMP(5);
    } else {
      for (u32 spins = 0; spins < (1u << 24); ++spins) {
        if (*ctl != 0u) break;
        __builtin_amdgcn_s_sleep(1);
      }
      __builtin_amdgcn_fence(__ATOMIC_ACQUIRE, "wavefront");
    }
    v4u pa[3], pb[3];
#pragma unroll
    for (int k = 0; k < 3; ++k) {
      const int ts = 2 * k + (w >> 1);
      const u32 po = (u32)(WS_PART + h * PART_HEAD + ts * 4096 + (w & 1) * 1024) + lane * 16;
      pa[k] = __builtin_amdgcn_raw_buffer_load_b128(rw, po, 0, 16);
      pb[k] = __builtin_amdgcn_raw_buffer_load_b128(rw, po + 2048, 0, 16);
    }
    const int e = w & 1;
    const int ch = lane & 7, be = (lane >> 3) & 1, ga = (lane >> 4) & 1, et = lane >> 5;
#pragma unroll
    for (int k = 0; k < 3; ++k) {
      const int ts = 2 * k + (w >> 1);
      const int tt = ts >= 3 ? 1 : 0, s = ts - 3 * tt;
      const f32x4 v = __builtin_bit_cast(f32x4, pa[k]) + __builtin_bit_cast(f32x4, pb[k]);
      const int d = 64 * s + 8 * ch + 4 * e + 2 * be + et;
      const u32 v01 = cvt_pk(v[0], v[1]), v23 = cvt_pk(v[2], v[3]);
      if (tt == 0) {
        LAS uint16_t* kp = (LAS uint16_t*)(L + KP_OFF + (4 * ga) * KP_STRIDE + 2 * d);
        kp[0] = (uint16_t)v01;
        kp[KP_STRIDE / 2] = (uint16_t)(v01 >> 16);
        kp[KP_STRIDE] = (uint16_t)v23;
        kp[3 * KP_STRIDE / 2] = (uint16_t)(v23 >> 16);
      } else {
        *(LAS v2u*)(L + VT_OFF + ((d & 7) >> 1) * 768 + (d >> 3) * 32 + ga * 16 + (d & 1) * 8) = v2u{v01, v23};
      }
    }
    STAMP(6);
    asm volatile("s_waitcnt vmcnt(0)" ::: "memory");
  }
  asm volatile("s_waitcnt lgkmcnt(0)" ::: "memory");
  __builtin_amdgcn_s_barrier();
  asm volatile("" ::: "memory");
  STAMP(7);

  v4u F[6];
#pragma unroll
  for (int s = 0; s < 6; ++s) F[s] = *(const LAS v4u*)(L + KP_OFF + (n & 7) * KP_STRIDE + 64 * s + 16 * g);
  f32x4 d0 = {0.f, 0.f, 0.f, 0.f}, d1 = {0.f, 0.f, 0.f, 0.f};
  const LAS unsigned char* qrow = L + Q_OFF + w * 12288 + n * 768;
#pragma unroll
  for (int s = 0; s < 6; ++s) {
    const int c0 = 4 * s + g, c1 = 24 + 4 * s + g;
    const v4u q0 = *(const LAS v4u*)(qrow + 16 * (c0 ^ n));
    const v4u q1 = *(const LAS v4u*)(qrow + 16 * (c1 ^ n));
    d0 = mfma32(F[s], q0, d0);
    d1 = mfma32(F[s], q1, d1);
  }
  v2u VB[3][8];
#pragma unroll
  for (int ga = 0; ga < 3; ++ga) {
    const int c = 16 * ga + n;
    const int pin = c >= 24 ? 1 : 0, cr = c - 24 * pin;
    const bool on = pin == (g >> 1);
#pragma unroll
    for (int i = 0; i < 4; ++i) {
      const v4u v = *(const LAS v4u*)(L + VT_OFF + i * 768 + cr * 32 + (g & 1) * 16);
      VB[ga][2 * i] = on ? v2u{v[0], v[1]} : v2u{0u, 0u};
      VB[ga][2 * i + 1] = on ? v2u{v[2], v[3]} : v2u{0u, 0u};
    }
  }
  const float sc = 0.07216878364870322f * 1.4426950408889634f;
  float sv[4];
#pragma unroll
  for (int r = 0; r < 4; ++r) sv[r] = (lane < 32 ? d0[r] : d1[r]);
  float m = fmaxf(fmaxf(sv[0], sv[1]), fmaxf(sv[2], sv[3]));
  {
    auto tt = __builtin_amdgcn_permlane16_swap(as_u(m), as_u(m), false, false);
    m = fmaxf(as_f(tt[0]), as_f(tt[1]));
  }
  float ps[4];
#pragma unroll
  for (int r = 0; r < 4; ++r) ps[r] = __builtin_amdgcn_exp2f((sv[r] - m) * sc);
  float sum = (ps[0] + ps[1]) + (ps[2] + ps[3]);
  {
    auto tt = __builtin_amdgcn_permlane16_swap(as_u(sum), as_u(sum), false, false);
    sum = as_f(tt[0]) + as_f(tt[1]);
  }
  const float inv = 1.0f / sum;
  const v2u pa2 = v2u{cvt_pk(ps[0] * inv, ps[1] * inv), cvt_pk(ps[2] * inv, ps[3] * inv)};
  const rsrc_t ro = __builtin_amdgcn_make_buffer_rsrc((void*)Op, 0, TENS_BYTES, 0x00020000);
  const u32 ob = qtile + (4 * g) * 768 + 16 * n;
#pragma unroll
  for (int ga = 0; ga < 3; ++ga) {
    f32x4 o[8];
#pragma unroll
    for (int i = 0; i < 8; ++i) o[i] = mfma16(pa2, VB[ga][i], f32x4{0.f, 0.f, 0.f, 0.f});
#pragma unroll
    for (int reg = 0; reg < 4; ++reg) {
      v4u v = {cvt_pk(o[0][reg], o[1][reg]), cvt_pk(o[2][reg], o[3][reg]), cvt_pk(o[4][reg], o[5][reg]), cvt_pk(o[6][reg], o[7][reg])};
      __builtin_amdgcn_raw_buffer_store_b128(v, ro, ob + reg * 768 + 256 * ga, 0, ST_AUX);
    }
  }
  STAMP(8);
}

extern "C" void kernel_launch(void* const* d_in, const int* in_sizes, int n_in, void* d_out, int out_size, void* d_ws, size_t ws_size,
                              hipStream_t stream) {
  (void)hipFuncSetAttribute((const void*)linformer_fused, hipFuncAttributeMaxDynamicSharedMemorySize, LDS_BYTES);
  linformer_fused<<<NWG, 512, LDS_BYTES, stream>>>((const uint16_t*)d_in[0], (const uint16_t*)d_in[1], (const uint16_t*)d_in[2],
                                                   (const uint16_t*)d_in[3], (const uint16_t*)d_in[4], (uint16_t*)d_out, (unsigned char*)d_ws);
}
```

```cpp
#include <hip/hip_runtime.h>
#include <hip/hip_bf16.h>
#include <stdint.h>

#define GAS __attribute__((address_space(1)))
#define LAS __attribute__((address_space(3)))
typedef unsigned int u32;
typedef unsigned long long u64;
typedef u32 v4u __attribute__((ext_vector_type(4)));
typedef u32 v2u __attribute__((ext_vector_type(2)));
typedef float f32x4 __attribute__((ext_vector_type(4)));
typedef float f32x2 __attribute__((ext_vector_type(2)));
typedef short s16x8 __attribute__((ext_vector_type(8)));
typedef short s16x4 __attribute__((ext_vector_type(4)));
typedef __bf16 bf16x2 __attribute__((ext_vector_type(2)));
typedef GAS u64 gu64;

constexpr int H = 32, S = 2048, D = 192, R = 8;
constexpr int ROWB = D * 2;
constexpr u32 TENS_BYTES = (u32)H * S * ROWB;
constexpr int NWG = 256;

#define QWIN 24
#define WX 4
#define RAMP 4
#define RAMP0 3
#define LOADER_PRIO 0
#define WQ WX
#define QAHEAD 3
#define SWEEP_DELAY 0
#define SENTINEL 0
#define PRETOUCH 1
#define POLL_SLEEP 2
#define PART_AUX 16
#define ST_AUX 18

constexpr size_t WS_EPOCH = 0;
#define FLAG_STRIDE 256
constexpr size_t WS_FLAG = 4096;
constexpr size_t WS_PART = WS_FLAG + 256 * 256;
constexpr size_t PART_HEAD = 12 * 2048;
constexpr size_t WS_PART_BYTES = H * PART_HEAD;
#define STAMP(i)

constexpr int Q_OFF = 0;
constexpr int RED_OFF = 98304;
constexpr int KP_OFF = RED_OFF + 32768;
constexpr int KP_STRIDE = 400;
constexpr int VT_OFF = KP_OFF + 8 * KP_STRIDE;
constexpr int CTL_OFF = VT_OFF + 192 * 16;
constexpr int SCR_OFF = CTL_OFF + 64;
constexpr int LDS_BYTES = SCR_OFF + 256;

__device__ __forceinline__ u32 as_u(float x) { return __builtin_bit_cast(u32, x); }
__device__ __forceinline__ float as_f(u32 x) { return __builtin_bit_cast(float, x); }
__device__ __forceinline__ f32x4 mfma32(v4u a, v4u b, f32x4 c) {
  return __builtin_amdgcn_mfma_f32_16x16x32_bf16(__builtin_bit_cast(s16x8, a), __builtin_bit_cast(s16x8, b), c, 0, 0, 0);
}
__device__ __forceinline__ f32x4 mfma16(v2u a, v2u b, f32x4 c) {
  return __builtin_amdgcn_mfma_f32_16x16x16bf16_1k(__builtin_bit_cast(s16x4, a), __builtin_bit_cast(s16x4, b), c, 0, 0, 0);
}
__device__ __forceinline__ u32 cvt_pk(float lo, float hi) {
  f32x2 v = {lo, hi};
  bf16x2 b = __builtin_convertvector(v, bf16x2);
  return __builtin_bit_cast(u32, b);
}
__device__ __forceinline__ float ror8(float x) {
  return as_f((u32)__builtin_amdgcn_update_dpp(0, (int)as_u(x), 0x128, 0xf, 0xf, true));
}
__device__ __forceinline__ float ror8_hi(float old, float x) {
  return as_f((u32)__builtin_amdgcn_update_dpp((int)as_u(old), (int)as_u(x), 0x128, 0xf, 0xc, false));
}
__device__ __forceinline__ void proj_pack(const f32x4 (&acc)[8], f32x4 (&z)[2]) {
#pragma unroll
  for (int e = 0; e < 2; ++e) {
#pragma unroll
    for (int reg = 0; reg < 4; ++reg) {
      auto s0 = __builtin_amdgcn_permlane32_swap(as_u(acc[4 * e + 0][reg]), as_u(acc[4 * e + 1][reg]), false, false);
      auto s1 = __builtin_amdgcn_permlane32_swap(as_u(acc[4 * e + 2][reg]), as_u(acc[4 * e + 3][reg]), false, false);
      const float x = as_f(s0[0]) + ror8(as_f(s0[1]));
      const float y = as_f(s1[0]) + ror8(as_f(s1[1]));
      z[e][reg] = ror8_hi(x, y);
    }
  }
}
typedef __amdgpu_buffer_rsrc_t rsrc_t;
template <int IMM> __device__ __forceinline__ void ld_nt(v4u& d, u32 voff, rsrc_t rs, u32 soff) {
  asm volatile("buffer_load_dwordx4 %0, %1, %2, %3 offen offset:%4 nt" : "=v"(d) : "v"(voff), "s"(rs), "s"(soff), "i"(IMM));
}
__device__ __forceinline__ void ld_plain(v4u& d, u32 voff, rsrc_t rs, u32 soff) {
  asm volatile("buffer_load_dwordx4 %0, %1, %2, %3 offen" : "=v"(d) : "v"(voff), "s"(rs), "s"(soff));
}
__device__ __forceinline__ void dma_nt(u32 lds_dst, u32 voff, rsrc_t rs, u32 soff) {
  asm volatile("s_mov_b32 m0, %0\n\ts_nop 0\n\tbuffer_load_dwordx4 %1, %2, %3 offen nt lds" ::"s"(lds_dst), "v"(voff), "s"(rs), "s"(soff) : "memory");
}
__device__ __forceinline__ u32 q_piece_off(int i, int g, int bb) {
  const int G = 4 * i + g, rho = (G * 43) >> 7;
  return (u32)(256 * G + 16 * (bb ^ rho));
}
template <int N> __device__ __forceinline__ void wait_vm() { asm volatile("s_waitcnt vmcnt(%0)" ::"i"(N) : "memory"); }

constexpr int NTHR = 512;
__device__ __forceinline__ u32 lds_ld(const LAS u32* p) { return (u32)__builtin_amdgcn_readfirstlane((int)__hip_atomic_load(p, __ATOMIC_RELAXED, __HIP_MEMORY_SCOPE_WORKGROUP)); }
__device__ __forceinline__ void lds_st(LAS u32* p, u32 v) { __hip_atomic_store(p, v, __ATOMIC_RELAXED, __HIP_MEMORY_SCOPE_WORKGROUP); }
__device__ __forceinline__ void lds_inc(LAS u32* p) { (void)__hip_atomic_fetch_add(p, 1u, __ATOMIC_RELAXED, __HIP_MEMORY_SCOPE_WORKGROUP); }
#define CFENCE() asm volatile("" ::: "memory")
enum { C_X = 0, C_KV = 1, C_DRAINED = 2, C_SEEN = 3 };

__global__ void __launch_bounds__(NTHR, 2)
linformer_fused(const uint16_t* __restrict__ Qp, const uint16_t* __restrict__ Kp, const uint16_t* __restrict__ Vp,
                const uint16_t* __restrict__ Ep, const uint16_t* __restrict__ Fp, uint16_t* __restrict__ Op, unsigned char* __restrict__ ws) {
  extern __shared__ __attribute__((aligned(16))) unsigned char smem[];
  LAS unsigned char* L = (LAS unsigned char*)smem;
  STAMP(0);
  const int b = blockIdx.x;
  const int h = (b & 7) * 4 + (b >> 6), j = (b >> 3) & 7;
  const int t = j >> 2, jj = j & 3;
  const int tid = threadIdx.x, lane = tid & 63, w = __builtin_amdgcn_readfirstlane(tid >> 6);
  const int g = lane >> 4, n = lane & 15;
  const u32 ldsb = (u32)(size_t)smem;
  LAS u32* ctl = (LAS u32*)(L + CTL_OFF);
  const u32 qtile = (u32)((h * S + 256 * j + 32 * w) * ROWB);
  const rsrc_t ro = __builtin_amdgcn_make_buffer_rsrc((void*)Op, 0, TENS_BYTES, 0x00020000);
  const rsrc_t rq = __builtin_amdgcn_make_buffer_rsrc((void*)Qp, 0, TENS_BYTES, 0x00020000);
  const rsrc_t rx = __builtin_amdgcn_make_buffer_rsrc((void*)(t ? Vp : Kp), 0, TENS_BYTES, 0x00020000);
  const rsrc_t rp = __builtin_amdgcn_make_buffer_rsrc((void*)(t ? Fp : Ep), 0, R * S * 2, 0x00020000);
  const rsrc_t rw = __builtin_amdgcn_make_buffer_rsrc((void*)ws, 0, (u32)(WS_PART + WS_PART_BYTES), 0x00020000);

  v2u ep;
  const int u0 = 24 * jj + 3 * w;
  const u32 voffE = (u32)((lane & 7) * (S * 2) + (16 * g + 8 * ((lane >> 3) & 1)) * 2);
  const u32 voffX = (u32)((16 * g + 8 * ((lane >> 3) & 1)) * ROWB + 16 * (lane & 7));
  v4u ef[3], x[3][8];
  u32 so[3];
#pragma unroll
  for (int i = 0; i < 3; ++i) so[i] = (u32)((h * S + ((u0 + i) & 31) * 64) * ROWB + 128 * ((u0 + i) >> 5));
#define LDX(i, k) { wait_vm<WX - 1>(); ld_nt<(k) * ROWB>(x[i][k], voffX, rx, so[i]); }
#define LDE(i) { wait_vm<WX - 1>(); ld_plain(ef[i], voffE, rp, (u32)(((u0 + i) & 31) * 128)); }
#define LDQA(i) { dma_nt(ldsb + Q_OFF + w * 12288 + (i) * 1024, q_piece_off((i), g, n), rq, qtile); }
#define LDQ(i) { wait_vm<WX - 1>(); dma_nt(ldsb + Q_OFF + w * 12288 + (i) * 1024, q_piece_off((i), g, n), rq, qtile); }
  ld_nt<0>(x[0][0], voffX, rx, so[0]); ld_nt<ROWB>(x[0][1], voffX, rx, so[0]);
  ld_plain(ef[0], voffE, rp, (u32)((u0 & 31) * 128));
  if (w == 0) {
    if (lane < 8) lds_st(ctl + lane, 0u);
    asm volatile("s_mov_b32 m0, %0\n\ts_nop 0\n\tbuffer_load_dword %1, %2, %3 offen nt lds" ::"s"(ldsb + SCR_OFF), "v"((u32)((lane & 1) * 98300)), "s"(ro), "s"(qtile) : "memory");
    asm volatile("s_mov_b32 m0, %0\n\ts_nop 0\n\tbuffer_load_dword %1, %2, %3 offen nt lds" ::"s"(ldsb + SCR_OFF), "v"((u32)((lane & 1) * 98300)), "s"(rq), "s"(qtile) : "memory");
  }
  const int slo = (24 * jj) >> 5;
  const int sA = u0 >> 5, sB = (u0 + 2) >> 5;
  LAS f32x4* red = (LAS f32x4*)(L + RED_OFF);
  {
    const f32x4 z0 = {0.f, 0.f, 0.f, 0.f};
    if (sA != slo) { red[(0 * 8 + w) * 128 + lane] = z0; red[(0 * 8 + w) * 128 + 64 + lane] = z0; }
    if (sB == slo) { red[(1 * 8 + w) * 128 + lane] = z0; red[(1 * 8 + w) * 128 + 64 + lane] = z0; }
  }
  asm volatile("s_waitcnt lgkmcnt(0)" ::: "memory");
  __builtin_amdgcn_s_barrier();
  CFENCE();
  LDX(0, 2) LDX(0, 3) LDX(0, 4) LDX(0, 5) LDX(0, 6) LDX(0, 7)
  LDE(1) LDX(1, 0) LDX(1, 1)

  f32x4 acc[8];
#define UNIT(i, ZERO)                                                                                                \
  _Pragma("unroll") for (int dd = 0; dd < 8; ++dd) {                                                                 \
    v4u f;                                                                                                           \
    _Pragma("unroll") for (int k = 0; k < 4; ++k)                                                                    \
        f[k] = __builtin_amdgcn_perm(x[i][2 * k + 1][dd >> 1], x[i][2 * k][dd >> 1], (dd & 1) ? 0x07060302u : 0x05040100u); \
    acc[dd] = mfma32(ef[i], f, ZERO ? f32x4{0.f, 0.f, 0.f, 0.f} : acc[dd]);                                          \
  }
#define FLUSH(strip)                                                                                                 \
  {                                                                                                                  \
    f32x4 z[2];                                                                                                      \
    proj_pack(acc, z);                                                                                               \
    const int sl = ((strip) != slo) ? 1 : 0;                                                                         \
    red[(sl * 8 + w) * 128 + lane] = z[0];                                                                           \
    red[(sl * 8 + w) * 128 + 64 + lane] = z[1];                                                                      \
  }
#define WAITX(i, N)                                                                                                  \
  asm volatile("s_waitcnt vmcnt(%9)"                                                                                 \
               : "+v"(x[i][0]), "+v"(x[i][1]), "+v"(x[i][2]), "+v"(x[i][3]), "+v"(x[i][4]), "+v"(x[i][5]), "+v"(x[i][6]), "+v"(x[i][7]), "+v"(ef[i]) \
               : "i"(N))
  WAITX(0, 3);
  LDX(1, 2)
  UNIT(0, true)
  const int s1 = (u0 + 1) >> 5;
  if (s1 != sA) {
    FLUSH(sA)
#pragma unroll
    for (int dd = 0; dd < 8; ++dd) acc[dd] = f32x4{0.f, 0.f, 0.f, 0.f};
  }
  LDX(1, 3) LDX(1, 4) LDX(1, 5) LDX(1, 6) LDX(1, 7)
  LDE(2) LDX(2, 0) LDX(2, 1)
  WAITX(1, 3);
  LDX(2, 2)
  UNIT(1, false)
  if (sB != s1) {
    FLUSH(s1)
#pragma unroll
    for (int dd = 0; dd < 8; ++dd) acc[dd] = f32x4{0.f, 0.f, 0.f, 0.f};
  }
  LDX(2, 3) LDX(2, 4) LDX(2, 5) LDX(2, 6) LDX(2, 7)
  wait_vm<WX - 1>();
  asm volatile("buffer_load_dwordx2 %0, off, %1, %2 sc1" : "=v"(ep) : "s"(rw), "s"((u32)(WS_EPOCH + h * 128)));
  LDQ(0) LDQ(1)
  STAMP(10);
  WAITX(2, 3);
  STAMP(1);
  LDQ(2)
  UNIT(2, false)
  FLUSH(sB)
  asm volatile("s_waitcnt lgkmcnt(0)" ::: "memory");
  if (lane == 0) lds_inc(ctl + C_X);
  asm volatile("s_waitcnt vmcnt(%1)" : "+v"(ep) : "i"(QAHEAD));
  const u64 tag = (((u64)(u32)__builtin_amdgcn_readfirstlane(ep[1]) << 32) | (u32)__builtin_amdgcn_readfirstlane(ep[0])) + 1ull;
  gu64* flags = (gu64*)(ws + WS_FLAG + h * 8 * FLAG_STRIDE);

  auto publish = [&]() {
    CFENCE();
    STAMP(2);
    const int half = lane >> 5, which = (lane >> 4) & 1, it = 16 * w + (lane & 15);
    f32x4 v = red[(which * 8 + 4 * half + 0) * 128 + it];
#pragma unroll
    for (int k = 1; k < 4; ++k) v += red[(which * 8 + 4 * half + k) * 128 + it];
    f32x4 tot;
#pragma unroll
    for (int r = 0; r < 4; ++r) {
      auto sw = __builtin_amdgcn_permlane32_swap(as_u(v[r]), as_u(v[r]), false, false);
      tot[r] = as_f(sw[0]) + as_f(sw[1]);
    }
    const bool has_hi = (jj == 1 || jj == 2);
    const int slot = 6 * t + 2 * jj - (jj > 0 ? 1 : 0) + which;
    const u32 off = (half == 0 && (which == 0 || has_hi)) ? (u32)(WS_PART + h * PART_HEAD + slot * 2048 + it * 16) : 0xfffffff0u;
    __builtin_amdgcn_raw_buffer_store_b128(__builtin_bit_cast(v4u, tot), rw, off, 0, PART_AUX);
    STAMP(3);
  };
  auto signal = [&]() {
    if (lane == 0) {
      const u32 old = __hip_atomic_fetch_add(ctl + C_DRAINED, 1u, __ATOMIC_RELAXED, __HIP_MEMORY_SCOPE_WORKGROUP);
      if (old == 7u) __hip_atomic_store(flags + j * (FLAG_STRIDE / 8), tag, __ATOMIC_RELAXED, __HIP_MEMORY_SCOPE_AGENT);
    }
    STAMP(4);
  };
  int younger = -1;
  _Pragma("nounroll") for (int i = QAHEAD; i < 12; ++i) {
    younger = __builtin_amdgcn_readfirstlane(younger);
    if (younger < 0 && lds_ld(ctl + C_X) >= 8u) { publish(); younger = 0; }
    wait_vm<WQ - 1>();
    if (younger >= WQ - 1 && younger < 99) { signal(); younger = 99; }
    dma_nt(ldsb + Q_OFF + w * 12288 + i * 1024, q_piece_off(i, g, n), rq, qtile);
    if (younger >= 0 && younger < 99) ++younger;
  }
  younger = __builtin_amdgcn_readfirstlane(younger);
  if (younger < 0) {
    _Pragma("nounroll") for (u32 spins = 0; spins < (1u << 22); ++spins) {
      if (lds_ld(ctl + C_X) >= 8u) break;
      __builtin_amdgcn_s_sleep(1);
    }
    publish();
  }
  wait_vm<0>();
  if (younger < 99) signal();
  STAMP(12);
  if (w == 7) {
    const gu64* fp = flags + (lane & 7) * (FLAG_STRIDE / 8);
    u32 spins = 0;
    _Pragma("nounroll") for (; spins < (1u << 20); ++spins) {
      const u64 f0 = __hip_atomic_load(fp, __ATOMIC_RELAXED, __HIP_MEMORY_SCOPE_AGENT);
      if (__all(f0 == tag)) break;
      __builtin_amdgcn_s_sleep(POLL_SLEEP);
    }
    __builtin_amdgcn_fence(__ATOMIC_ACQUIRE, "wavefront");
    lds_st(ctl + C_SEEN, 1u);
    STAMP(5);
  }
  if (w < 6) {
    _Pragma("nounroll") for (u32 spins = 0; spins < (1u << 22); ++spins) {
      if (lds_ld(ctl + C_SEEN) != 0u) break;
      __builtin_amdgcn_s_sleep(1);
    }
    __builtin_amdgcn_fence(__ATOMIC_ACQUIRE, "wavefront");
    STAMP(5);
    f32x4 val[2][2];
#pragma unroll
    for (int p = 0; p < 2; ++p)
#pragma unroll
      for (int e = 0; e < 2; ++e)
        val[p][e] = __builtin_bit_cast(f32x4, __builtin_amdgcn_raw_buffer_load_b128(rw, (u32)(WS_PART + h * PART_HEAD + (2 * w + p) * 2048 + e * 1024) + lane * 16, 0, 16));
    const int tt = w >= 3 ? 1 : 0, s = w - 3 * tt;
    const int ch = lane & 7, be = (lane >> 3) & 1, ga = (lane >> 4) & 1, et = lane >> 5;
#pragma unroll
    for (int e = 0; e < 2; ++e) {
      const int d = 64 * s + 8 * ch + 4 * e + 2 * be + et;
      const u32 v01 = cvt_pk(val[0][e][0] + val[1][e][0], val[0][e][1] + val[1][e][1]);
      const u32 v23 = cvt_pk(val[0][e][2] + val[1][e][2], val[0][e][3] + val[1][e][3]);
      if (tt == 0) {
        LAS uint16_t* kp = (LAS uint16_t*)(L + KP_OFF + (4 * ga) * KP_STRIDE + 2 * d);
        kp[0] = (uint16_t)v01;
        kp[KP_STRIDE / 2] = (uint16_t)(v01 >> 16);
        kp[KP_STRIDE] = (uint16_t)v23;
        kp[3 * KP_STRIDE / 2] = (uint16_t)(v23 >> 16);
      } else {
        *(LAS v2u*)(L + VT_OFF + ((d & 7) >> 1) * 768 + (d >> 3) * 32 + ga * 16 + (d & 1) * 8) = v2u{v01, v23};
      }
    }
    asm volatile("s_waitcnt lgkmcnt(0)" ::: "memory");
    if (lane == 0) lds_inc(ctl + C_KV);
    STAMP(6);
  }
  _Pragma("nounroll") for (u32 spins = 0; spins < (1u << 22); ++spins) {
    if (lds_ld(ctl + C_KV) >= 6u) break;
    __builtin_amdgcn_s_sleep(1);
  }
  CFENCE();
  STAMP(7);
  if (j == 0 && tid == 0) __hip_atomic_store((gu64*)(ws + WS_EPOCH + h * 128), tag, __ATOMIC_RELAXED, __HIP_MEMORY_SCOPE_AGENT);

  v4u F[6];
#pragma unroll
  for (int s = 0; s < 6; ++s) F[s] = *(const LAS v4u*)(L + KP_OFF + (n & 7) * KP_STRIDE + 64 * s + 16 * g);
  f32x4 d0 = {0.f, 0.f, 0.f, 0.f}, d1 = {0.f, 0.f, 0.f, 0.f};
  const LAS unsigned char* qrow = L + Q_OFF + w * 12288 + n * 768;
#pragma unroll
  for (int s = 0; s < 6; ++s) {
    const int c0 = 4 * s + g, c1 = 24 + 4 * s + g;
    const v4u q0 = *(const LAS v4u*)(qrow + 16 * (c0 ^ n));
    const v4u q1 = *(const LAS v4u*)(qrow + 16 * (c1 ^ n));
    d0 = mfma32(F[s], q0, d0);
    d1 = mfma32(F[s], q1, d1);
  }
  v2u VB[3][8];
#pragma unroll
  for (int ga = 0; ga < 3; ++ga) {
    const int c = 16 * ga + n;
    const int pin = c >= 24 ? 1 : 0, cr = c - 24 * pin;
    const bool on = pin == (g >> 1);
#pragma unroll
    for (int i = 0; i < 4; ++i) {
      const v4u v = *(const LAS v4u*)(L + VT_OFF + i * 768 + cr * 32 + (g & 1) * 16);
      VB[ga][2 * i] = on ? v2u{v[0], v[1]} : v2u{0u, 0u};
      VB[ga][2 * i + 1] = on ? v2u{v[2], v[3]} : v2u{0u, 0u};
    }
  }
  const float sc = 0.07216878364870322f * 1.4426950408889634f;
  float sv[4];
#pragma unroll
  for (int r = 0; r < 4; ++r) sv[r] = (lane < 32 ? d0[r] : d1[r]);
  float m = fmaxf(fmaxf(sv[0], sv[1]), fmaxf(sv[2], sv[3]));
  {
    auto tt = __builtin_amdgcn_permlane16_swap(as_u(m), as_u(m), false, false);
    m = fmaxf(as_f(tt[0]), as_f(tt[1]));
  }
  float ps[4];
#pragma unroll
  for (int r = 0; r < 4; ++r) ps[r] = __builtin_amdgcn_exp2f((sv[r] - m) * sc);
  float sum = (ps[0] + ps[1]) + (ps[2] + ps[3]);
  {
    auto tt = __builtin_amdgcn_permlane16_swap(as_u(sum), as_u(sum), false, false);
    sum = as_f(tt[0]) + as_f(tt[1]);
  }
  const float inv = 1.0f / sum;
  const v2u pa2 = v2u{cvt_pk(ps[0] * inv, ps[1] * inv), cvt_pk(ps[2] * inv, ps[3] * inv)};
  const u32 ob = qtile + (4 * g) * 768 + 16 * n;
#pragma unroll
  for (int ga = 0; ga < 3; ++ga) {
    f32x4 o[8];
#pragma unroll
    for (int i = 0; i < 8; ++i) o[i] = mfma16(pa2, VB[ga][i], f32x4{0.f, 0.f, 0.f, 0.f});
#pragma unroll
    for (int reg = 0; reg < 4; ++reg) {
      v4u v = {cvt_pk(o[0][reg], o[1][reg]), cvt_pk(o[2][reg], o[3][reg]), cvt_pk(o[4][reg], o[5][reg]), cvt_pk(o[6][reg], o[7][reg])};
      __builtin_amdgcn_raw_buffer_store_b128(v, ro, ob + reg * 768 + 256 * ga, 0, ST_AUX);
    }
  }
  STAMP(8);
}

extern "C" void kernel_launch(void* const* d_in, const int* in_sizes, int n_in, void* d_out, int out_size, void* d_ws, size_t ws_size,
                              hipStream_t stream) {
  (void)hipFuncSetAttribute((const void*)linformer_fused, hipFuncAttributeMaxDynamicSharedMemorySize, LDS_BYTES);
  linformer_fused<<<NWG, NTHR, LDS_BYTES, stream>>>((const uint16_t*)d_in[0], (const uint16_t*)d_in[1], (const uint16_t*)d_in[2],
                                                   (const uint16_t*)d_in[3], (const uint16_t*)d_in[4], (uint16_t*)d_out, (unsigned char*)d_ws);
}
```

```cpp
#include <hip/hip_runtime.h>
#include <hip/hip_bf16.h>
#include <stdint.h>

#define GAS __attribute__((address_space(1)))
#define LAS __attribute__((address_space(3)))
typedef unsigned int u32;
typedef unsigned long long u64;
typedef u32 v4u __attribute__((ext_vector_type(4)));
typedef u32 v2u __attribute__((ext_vector_type(2)));
typedef float f32x4 __attribute__((ext_vector_type(4)));
typedef float f32x2 __attribute__((ext_vector_type(2)));
typedef short s16x8 __attribute__((ext_vector_type(8)));
typedef short s16x4 __attribute__((ext_vector_type(4)));
typedef __bf16 bf16x2 __attribute__((ext_vector_type(2)));
typedef GAS u64 gu64;

constexpr int H = 32, S = 2048, D = 192, R = 8;
constexpr int ROWB = D * 2;
constexpr u32 TENS_BYTES = (u32)H * S * ROWB;
constexpr int NWG = 256;

#define QWIN 24
#define WX 4
#define RAMP 4
#define RAMP0 3
#define LOADER_PRIO 0
#define RETRY_SLEEP 6
#define G16 1
#define SWEEP_AT 12
#define WQ WX
#define QAHEAD 3
#define SWEEP_DELAY 0
#define SENTINEL 0
#define PRETOUCH 1
#define POLL_SLEEP 2
#define PART_AUX 16
#define ST_AUX 18

constexpr size_t WS_EPOCH = 0;
constexpr size_t WS_PART = 4096;
constexpr size_t PART_HEAD = 12 * 2048;
constexpr size_t WS_PART_BYTES = H * PART_HEAD;
#define STAMP(i)

constexpr int Q_OFF = 0;
constexpr int RED_OFF = 98304;
constexpr int KP_OFF = RED_OFF + 32768;
constexpr int KP_STRIDE = 400;
constexpr int VT_OFF = KP_OFF + 8 * KP_STRIDE;
constexpr int CTL_OFF = VT_OFF + 192 * 16;
constexpr int SCR_OFF = CTL_OFF + 64;
constexpr int LDS_BYTES = SCR_OFF + 256;

__device__ __forceinline__ u32 as_u(float x) { return __builtin_bit_cast(u32, x); }
__device__ __forceinline__ float as_f(u32 x) { return __builtin_bit_cast(float, x); }
__device__ __forceinline__ f32x4 mfma32(v4u a, v4u b, f32x4 c) {
  return __builtin_amdgcn_mfma_f32_16x16x32_bf16(__builtin_bit_cast(s16x8, a), __builtin_bit_cast(s16x8, b), c, 0, 0, 0);
}
__device__ __forceinline__ f32x4 mfma16(v2u a, v2u b, f32x4 c) {
  return __builtin_amdgcn_mfma_f32_16x16x16bf16_1k(__builtin_bit_cast(s16x4, a), __builtin_bit_cast(s16x4, b), c, 0, 0, 0);
}
__device__ __forceinline__ u32 cvt_pk(float lo, float hi) {
  f32x2 v = {lo, hi};
  bf16x2 b = __builtin_convertvector(v, bf16x2);
  return __builtin_bit_cast(u32, b);
}
__device__ __forceinline__ float ror8(float x) {
  return as_f((u32)__builtin_amdgcn_update_dpp(0, (int)as_u(x), 0x128, 0xf, 0xf, true));
}
__device__ __forceinline__ float ror8_hi(float old, float x) {
  return as_f((u32)__builtin_amdgcn_update_dpp((int)as_u(old), (int)as_u(x), 0x128, 0xf, 0xc, false));
}
__device__ __forceinline__ void proj_pack(const f32x4 (&acc)[8], f32x4 (&z)[2]) {
#pragma unroll
  for (int e = 0; e < 2; ++e) {
#pragma unroll
    for (int reg = 0; reg < 4; ++reg) {
      auto s0 = __builtin_amdgcn_permlane32_swap(as_u(acc[4 * e + 0][reg]), as_u(acc[4 * e + 1][reg]), false, false);
      auto s1 = __builtin_amdgcn_permlane32_swap(as_u(acc[4 * e + 2][reg]), as_u(acc[4 * e + 3][reg]), false, false);
      const float x = as_f(s0[0]) + ror8(as_f(s0[1]));
      const float y = as_f(s1[0]) + ror8(as_f(s1[1]));
      z[e][reg] = ror8_hi(x, y);
    }
  }
}
typedef __amdgpu_buffer_rsrc_t rsrc_t;
template <int IMM> __device__ __forceinline__ void ld_nt(v4u& d, u32 voff, rsrc_t rs, u32 soff) {
  asm volatile("buffer_load_dwordx4 %0, %1, %2, %3 offen offset:%4 nt" : "=v"(d) : "v"(voff), "s"(rs), "s"(soff), "i"(IMM));
}
__device__ __forceinline__ void ld_plain(v4u& d, u32 voff, rsrc_t rs, u32 soff) {
  asm volatile("buffer_load_dwordx4 %0, %1, %2, %3 offen" : "=v"(d) : "v"(voff), "s"(rs), "s"(soff));
}
__device__ __forceinline__ void dma_nt(u32 lds_dst, u32 voff, rsrc_t rs, u32 soff) {
  asm volatile("s_mov_b32 m0, %0\n\ts_nop 0\n\tbuffer_load_dwordx4 %1, %2, %3 offen nt lds" ::"s"(lds_dst), "v"(voff), "s"(rs), "s"(soff) : "memory");
}
__device__ __forceinline__ u32 q_piece_off(int i, int g, int bb) {
  const int G = 4 * i + g, rho = (G * 43) >> 7;
  return (u32)(256 * G + 16 * (bb ^ rho));
}
template <int N> __device__ __forceinline__ void wait_vm() { asm volatile("s_waitcnt vmcnt(%0)" ::"i"(N) : "memory"); }

constexpr int NTHR = 512;
__device__ __forceinline__ u32 lds_ld(const LAS u32* p) { return (u32)__builtin_amdgcn_readfirstlane((int)__hip_atomic_load(p, __ATOMIC_RELAXED, __HIP_MEMORY_SCOPE_WORKGROUP)); }
__device__ __forceinline__ void lds_st(LAS u32* p, u32 v) { __hip_atomic_store(p, v, __ATOMIC_RELAXED, __HIP_MEMORY_SCOPE_WORKGROUP); }
__device__ __forceinline__ void lds_inc(LAS u32* p) { (void)__hip_atomic_fetch_add(p, 1u, __ATOMIC_RELAXED, __HIP_MEMORY_SCOPE_WORKGROUP); }
#define CFENCE() asm volatile("" ::: "memory")
enum { C_X = 0, C_KV = 1 };

__global__ void __launch_bounds__(NTHR, 2)
linformer_fused(const uint16_t* __restrict__ Qp, const uint16_t* __restrict__ Kp, const uint16_t* __restrict__ Vp,
                const uint16_t* __restrict__ Ep, const uint16_t* __restrict__ Fp, uint16_t* __restrict__ Op, unsigned char* __restrict__ ws) {
  extern __shared__ __attribute__((aligned(16))) unsigned char smem[];
  LAS unsigned char* L = (LAS unsigned char*)smem;
  STAMP(0);
  const int b = blockIdx.x;
  const int h = (b & 7) * 4 + (b >> 6), j = (b >> 3) & 7;
  const int t = j >> 2, jj = j & 3;
  const int tid = threadIdx.x, lane = tid & 63, w = __builtin_amdgcn_readfirstlane(tid >> 6);
  const int g = lane >> 4, n = lane & 15;
  const u32 ldsb = (u32)(size_t)smem;
  LAS u32* ctl = (LAS u32*)(L + CTL_OFF);
  const u32 qtile = (u32)((h * S + 256 * j + 32 * w) * ROWB);
  const rsrc_t ro = __builtin_amdgcn_make_buffer_rsrc((void*)Op, 0, TENS_BYTES, 0x00020000);
  const rsrc_t rq = __builtin_amdgcn_make_buffer_rsrc((void*)Qp, 0, TENS_BYTES, 0x00020000);
  const rsrc_t rx = __builtin_amdgcn_make_buffer_rsrc((void*)(t ? Vp : Kp), 0, TENS_BYTES, 0x00020000);
  const rsrc_t rp = __builtin_amdgcn_make_buffer_rsrc((void*)(t ? Fp : Ep), 0, R * S * 2, 0x00020000);
  const rsrc_t rw = __builtin_amdgcn_make_buffer_rsrc((void*)ws, 0, (u32)(WS_PART + WS_PART_BYTES), 0x00020000);

  u32 ep;
  const int u0 = 24 * jj + 3 * w;
  const u32 voffE = (u32)((lane & 7) * (S * 2) + (16 * g + 8 * ((lane >> 3) & 1)) * 2);
  const u32 voffX = (u32)((16 * g + 8 * ((lane >> 3) & 1)) * ROWB + 16 * (lane & 7));
  v4u ef[3], x[3][8];
  u32 so[3];
#pragma unroll
  for (int i = 0; i < 3; ++i) so[i] = (u32)((h * S + ((u0 + i) & 31) * 64) * ROWB + 128 * ((u0 + i) >> 5));
#define LDX(i, k) { wait_vm<WX - 1>(); ld_nt<(k) * ROWB>(x[i][k], voffX, rx, so[i]); }
#define LDE(i) { wait_vm<WX - 1>(); ld_plain(ef[i], voffE, rp, (u32)(((u0 + i) & 31) * 128)); }
#define LDQA(i) { dma_nt(ldsb + Q_OFF + w * 12288 + (i) * 1024, q_piece_off((i), g, n), rq, qtile); }
#define LDQ(i) { wait_vm<WX - 1>(); dma_nt(ldsb + Q_OFF + w * 12288 + (i) * 1024, q_piece_off((i), g, n), rq, qtile); }
  ld_nt<0>(x[0][0], voffX, rx, so[0]); ld_nt<ROWB>(x[0][1], voffX, rx, so[0]);
  ld_plain(ef[0], voffE, rp, (u32)((u0 & 31) * 128));
  if (w == 0) {
    if (lane < 8) lds_st(ctl + lane, 0u);
    asm volatile("s_mov_b32 m0, %0\n\ts_nop 0\n\tbuffer_load_dword %1, %2, %3 offen nt lds" ::"s"(ldsb + SCR_OFF), "v"((u32)((lane & 1) * 98300)), "s"(ro), "s"(qtile) : "memory");
    asm volatile("s_mov_b32 m0, %0\n\ts_nop 0\n\tbuffer_load_dword %1, %2, %3 offen nt lds" ::"s"(ldsb + SCR_OFF), "v"((u32)((lane & 1) * 98300)), "s"(rq), "s"(qtile) : "memory");
  }
  const int slo = (24 * jj) >> 5;
  const int sA = u0 >> 5, sB = (u0 + 2) >> 5;
  LAS f32x4* red = (LAS f32x4*)(L + RED_OFF);
  {
    const f32x4 z0 = {0.f, 0.f, 0.f, 0.f};
    if (sA != slo) { red[(0 * 8 + w) * 128 + lane] = z0; red[(0 * 8 + w) * 128 + 64 + lane] = z0; }
    if (sB == slo) { red[(1 * 8 + w) * 128 + lane] = z0; red[(1 * 8 + w) * 128 + 64 + lane] = z0; }
  }
  asm volatile("s_waitcnt lgkmcnt(0)" ::: "memory");
  __builtin_amdgcn_s_barrier();
  CFENCE();
  LDX(0, 2) LDX(0, 3) LDX(0, 4) LDX(0, 5) LDX(0, 6) LDX(0, 7)
  LDE(1) LDX(1, 0) LDX(1, 1)

  f32x4 acc[8];
#define UNIT(i, ZERO)                                                                                                \
  _Pragma("unroll") for (int dd = 0; dd < 8; ++dd) {                                                                 \
    v4u f;                                                                                                           \
    _Pragma("unroll") for (int k = 0; k < 4; ++k)                                                                    \
        f[k] = __builtin_amdgcn_perm(x[i][2 * k + 1][dd >> 1], x[i][2 * k][dd >> 1], (dd & 1) ? 0x07060302u : 0x05040100u); \
    acc[dd] = mfma32(ef[i], f, ZERO ? f32x4{0.f, 0.f, 0.f, 0.f} : acc[dd]);                                          \
  }
#define FLUSH(strip)                                                                                                 \
  {                                                                                                                  \
    f32x4 z[2];                                                                                                      \
    proj_pack(acc, z);                                                                                               \
    const int sl = ((strip) != slo) ? 1 : 0;                                                                         \
    red[(sl * 8 + w) * 128 + lane] = z[0];                                                                           \
    red[(sl * 8 + w) * 128 + 64 + lane] = z[1];                                                                      \
  }
#define WAITX(i, N)                                                                                                  \
  asm volatile("s_waitcnt vmcnt(%9)"                                                                                 \
               : "+v"(x[i][0]), "+v"(x[i][1]), "+v"(x[i][2]), "+v"(x[i][3]), "+v"(x[i][4]), "+v"(x[i][5]), "+v"(x[i][6]), "+v"(x[i][7]), "+v"(ef[i]) \
               : "i"(N))
  WAITX(0, 3);
  LDX(1, 2)
  UNIT(0, true)
  const int s1 = (u0 + 1) >> 5;
  if (s1 != sA) {
    FLUSH(sA)
#pragma unroll
    for (int dd = 0; dd < 8; ++dd) acc[dd] = f32x4{0.f, 0.f, 0.f, 0.f};
  }
  LDX(1, 3) LDX(1, 4) LDX(1, 5) LDX(1, 6) LDX(1, 7)
  LDE(2) LDX(2, 0) LDX(2, 1)
  WAITX(1, 3);
  LDX(2, 2)
  UNIT(1, false)
  if (sB != s1) {
    FLUSH(s1)
#pragma unroll
    for (int dd = 0; dd < 8; ++dd) acc[dd] = f32x4{0.f, 0.f, 0.f, 0.f};
  }
  LDX(2, 3) LDX(2, 4) LDX(2, 5) LDX(2, 6) LDX(2, 7)
  wait_vm<WX - 1>();
  asm volatile("buffer_load_dword %0, off, %1, %2 sc1" : "=v"(ep) : "s"(rw), "s"((u32)(WS_EPOCH + h * 128)));
  LDQ(0) LDQ(1)
  STAMP(10);
  WAITX(2, 3);
  STAMP(1);
  LDQ(2)
  UNIT(2, false)
  FLUSH(sB)
  asm volatile("s_waitcnt lgkmcnt(0)" ::: "memory");
  if (lane == 0) lds_inc(ctl + C_X);
  asm volatile("s_waitcnt vmcnt(%1)" : "+v"(ep) : "i"(QAHEAD));
  const u32 tag = (u32)__builtin_amdgcn_readfirstlane(ep) + 1u;
  gu64* gran = (gu64*)(ws + WS_PART + h * PART_HEAD);

  auto publish = [&]() {
    CFENCE();
    STAMP(2);
    const int half = lane >> 5, which = (lane >> 4) & 1, it = 16 * w + (lane & 15);
    f32x4 v = red[(which * 8 + 4 * half + 0) * 128 + it];
#pragma unroll
    for (int k = 1; k < 4; ++k) v += red[(which * 8 + 4 * half + k) * 128 + it];
    f32x4 tot;
#pragma unroll
    for (int r = 0; r < 4; ++r) {
      auto sw = __builtin_amdgcn_permlane32_swap(as_u(v[r]), as_u(v[r]), false, false);
      tot[r] = as_f(sw[0]) + as_f(sw[1]);
    }
    const bool has_hi = (jj == 1 || jj == 2);
    const int slot = 6 * t + 2 * jj - (jj > 0 ? 1 : 0) + which;
    const u32 off = (half == 0 && (which == 0 || has_hi)) ? (u32)(WS_PART + h * PART_HEAD + slot * 2048 + it * 16) : 0xfffffff0u;
    __builtin_amdgcn_raw_buffer_store_b128(v4u{cvt_pk(tot[0], tot[1]), tag, cvt_pk(tot[2], tot[3]), tag}, rw, off, 0, 16);
    STAMP(3);
  };
  int published = 0;
  _Pragma("nounroll") for (int i = QAHEAD; i < 12; ++i) {
    published = __builtin_amdgcn_readfirstlane(published);
    if (!published && lds_ld(ctl + C_X) >= 8u) { publish(); published = 1; }
    wait_vm<WQ - 1>();
    dma_nt(ldsb + Q_OFF + w * 12288 + i * 1024, q_piece_off(i, g, n), rq, qtile);
  }
  published = __builtin_amdgcn_readfirstlane(published);
  if (!published) {
    _Pragma("nounroll") for (u32 spins = 0; spins < (1u << 22); ++spins) {
      if (lds_ld(ctl + C_X) >= 8u) break;
      __builtin_amdgcn_s_sleep(1);
    }
    publish();
  }
  if (w < 6) {
    STAMP(4);
    u32 val[2][2][2];
    u32 spins = 0;
    {
      v4u gr[2][2];
      const u32 po = (u32)(WS_PART + h * PART_HEAD + (2 * w) * 2048) + lane * 16;
#pragma unroll
      for (int p = 0; p < 2; ++p)
#pragma unroll
        for (int e = 0; e < 2; ++e) gr[p][e] = __builtin_amdgcn_raw_buffer_load_b128(rw, po + p * 2048 + e * 1024, 0, 16);
      _Pragma("nounroll") for (; spins < (1u << 20); ++spins) {
        bool ok[2][2], all = true;
#pragma unroll
        for (int p = 0; p < 2; ++p)
#pragma unroll
          for (int e = 0; e < 2; ++e) { ok[p][e] = (gr[p][e][1] == tag) & (gr[p][e][3] == tag); all &= ok[p][e]; }
        if (__all(all)) break;
        __builtin_amdgcn_s_sleep(RETRY_SLEEP);
#pragma unroll
        for (int p = 0; p < 2; ++p)
#pragma unroll
          for (int e = 0; e < 2; ++e)
            if (!ok[p][e]) gr[p][e] = __builtin_amdgcn_raw_buffer_load_b128(rw, po + p * 2048 + e * 1024, 0, 16);
      }
#pragma unroll
      for (int p = 0; p < 2; ++p)
#pragma unroll
        for (int e = 0; e < 2; ++e) { val[p][0][e] = gr[p][e][0]; val[p][1][e] = gr[p][e][2]; }
    }
    wait_vm<0>();
    STAMP(5);
    const int tt = w >= 3 ? 1 : 0, s = w - 3 * tt;
    const int ch = lane & 7, be = (lane >> 3) & 1, ga = (lane >> 4) & 1, et = lane >> 5;
#pragma unroll
    for (int e = 0; e < 2; ++e) {
      const int d = 64 * s + 8 * ch + 4 * e + 2 * be + et;
      const u32 v01 = cvt_pk(as_f(val[0][0][e] << 16) + as_f(val[1][0][e] << 16), as_f(val[0][0][e] & 0xffff0000u) + as_f(val[1][0][e] & 0xffff0000u));
      const u32 v23 = cvt_pk(as_f(val[0][1][e] << 16) + as_f(val[1][1][e] << 16), as_f(val[0][1][e] & 0xffff0000u) + as_f(val[1][1][e] & 0xffff0000u));
      if (tt == 0) {
        LAS uint16_t* kp = (LAS uint16_t*)(L + KP_OFF + (4 * ga) * KP_STRIDE + 2 * d);
        kp[0] = (uint16_t)v01;
        kp[KP_STRIDE / 2] = (uint16_t)(v01 >> 16);
        kp[KP_STRIDE] = (uint16_t)v23;
        kp[3 * KP_STRIDE / 2] = (uint16_t)(v23 >> 16);
      } else {
        *(LAS v2u*)(L + VT_OFF + ((d & 7) >> 1) * 768 + (d >> 3) * 32 + ga * 16 + (d & 1) * 8) = v2u{v01, v23};
      }
    }
    asm volatile("s_waitcnt lgkmcnt(0)" ::: "memory");
    if (lane == 0) lds_inc(ctl + C_KV);
    STAMP(6);
  } else {
    wait_vm<0>();
  }
  STAMP(12);
  _Pragma("nounroll") for (u32 spins = 0; spins < (1u << 22); ++spins) {
    if (lds_ld(ctl + C_KV) >= 6u) break;
    __builtin_amdgcn_s_sleep(1);
  }
  CFENCE();
  STAMP(7);
  if (j == 0 && tid == 0) __hip_atomic_store((GAS u32*)(ws + WS_EPOCH + h * 128), tag, __ATOMIC_RELAXED, __HIP_MEMORY_SCOPE_AGENT);

  v4u F[6];
#pragma unroll
  for (int s = 0; s < 6; ++s) F[s] = *(const LAS v4u*)(L + KP_OFF + (n & 7) * KP_STRIDE + 64 * s + 16 * g);
  f32x4 d0 = {0.f, 0.f, 0.f, 0.f}, d1 = {0.f, 0.f, 0.f, 0.f};
  const LAS unsigned char* qrow = L + Q_OFF + w * 12288 + n * 768;
#pragma unroll
  for (int s = 0; s < 6; ++s) {
    const int c0 = 4 * s + g, c1 = 24 + 4 * s + g;
    const v4u q0 = *(const LAS v4u*)(qrow + 16 * (c0 ^ n));
    const v4u q1 = *(const LAS v4u*)(qrow + 16 * (c1 ^ n));
    d0 = mfma32(F[s], q0, d0);
    d1 = mfma32(F[s], q1, d1);
  }
  v2u VB[3][8];
#pragma unroll
  for (int ga = 0; ga < 3; ++ga) {
    const int c = 16 * ga + n;
    const int pin = c >= 24 ? 1 : 0, cr = c - 24 * pin;
    const bool on = pin == (g >> 1);
#pragma unroll
    for (int i = 0; i < 4; ++i) {
      const v4u v = *(const LAS v4u*)(L + VT_OFF + i * 768 + cr * 32 + (g & 1) * 16);
      VB[ga][2 * i] = on ? v2u{v[0], v[1]} : v2u{0u, 0u};
      VB[ga][2 * i + 1] = on ? v2u{v[2], v[3]} : v2u{0u, 0u};
    }
  }
  const float sc = 0.07216878364870322f * 1.4426950408889634f;
  float sv[4];
#pragma unroll
  for (int r = 0; r < 4; ++r) sv[r] = (lane < 32 ? d0[r] : d1[r]);
  float m = fmaxf(fmaxf(sv[0], sv[1]), fmaxf(sv[2], sv[3]));
  {
    auto tt = __builtin_amdgcn_permlane16_swap(as_u(m), as_u(m), false, false);
    m = fmaxf(as_f(tt[0]), as_f(tt[1]));
  }
  float ps[4];
#pragma unroll
  for (int r = 0; r < 4; ++r) ps[r] = __builtin_amdgcn_exp2f((sv[r] - m) * sc);
  float sum = (ps[0] + ps[1]) + (ps[2] + ps[3]);
  {
    auto tt = __builtin_amdgcn_permlane16_swap(as_u(sum), as_u(sum), false, false);
    sum = as_f(tt[0]) + as_f(tt[1]);
  }
  const float inv = 1.0f / sum;
  const v2u pa2 = v2u{cvt_pk(ps[0] * inv, ps[1] * inv), cvt_pk(ps[2] * inv, ps[3] * inv)};
  const u32 ob = qtile + (4 * g) * 768 + 16 * n;
#pragma unroll
  for (int ga = 0; ga < 3; ++ga) {
    f32x4 o[8];
#pragma unroll
    for (int i = 0; i < 8; ++i) o[i] = mfma16(pa2, VB[ga][i], f32x4{0.f, 0.f, 0.f, 0.f});
#pragma unroll
    for (int reg = 0; reg < 4; ++reg) {
      v4u v = {cvt_pk(o[0][reg], o[1][reg]), cvt_pk(o[2][reg], o[3][reg]), cvt_pk(o[4][reg], o[5][reg]), cvt_pk(o[6][reg], o[7][reg])};
      __builtin_amdgcn_raw_buffer_store_b128(v, ro, ob + reg * 768 + 256 * ga, 0, ST_AUX);
    }
  }
  STAMP(8);
}

extern "C" void kernel_launch(void* const* d_in, const int* in_sizes, int n_in, void* d_out, int out_size, void* d_ws, size_t ws_size,
                              hipStream_t stream) {
  (void)hipFuncSetAttribute((const void*)linformer_fused, hipFuncAttributeMaxDynamicSharedMemorySize, LDS_BYTES);
  linformer_fused<<<NWG, NTHR, LDS_BYTES, stream>>>((const uint16_t*)d_in[0], (const uint16_t*)d_in[1], (const uint16_t*)d_in[2],
                                                   (const uint16_t*)d_in[3], (const uint16_t*)d_in[4], (uint16_t*)d_out, (unsigned char*)d_ws);
}
```

```cpp
#include <hip/hip_runtime.h>
#include <hip/hip_bf16.h>
#include <stdint.h>

#define GAS __attribute__((address_space(1)))
#define LAS __attribute__((address_space(3)))
typedef unsigned int u32;
typedef unsigned long long u64;
typedef u32 v4u __attribute__((ext_vector_type(4)));
typedef u32 v2u __attribute__((ext_vector_type(2)));
typedef float f32x4 __attribute__((ext_vector_type(4)));
typedef float f32x2 __attribute__((ext_vector_type(2)));
typedef short s16x8 __attribute__((ext_vector_type(8)));
typedef short s16x4 __attribute__((ext_vector_type(4)));
typedef __bf16 bf16x2 __attribute__((ext_vector_type(2)));
typedef GAS u64 gu64;

constexpr int H = 32, S = 2048, D = 192, R = 8;
constexpr int ROWB = D * 2;
constexpr u32 TENS_BYTES = (u32)H * S * ROWB;
constexpr int NWG = 256;

#define QWIN 24
#define WX 4
#define RAMP 4
#define RAMP0 3
#define LOADER_PRIO 0
#define RETRY_SLEEP 6
#define G16 1
#define SWEEP_AT 12
#define WQ WX
#define QAHEAD 3
#define SWEEP_DELAY 0
#define SENTINEL 0
#define PRETOUCH 1
#define POLL_SLEEP 2
#define PART_AUX 16
#define PART_LD_AUX 16
#define ST_AUX 18

constexpr size_t WS_EPOCH = 0;
constexpr size_t WS_PART = 4096;
#define F32PART 0
constexpr size_t SLOT_B = F32PART ? 4096 : 2048;
constexpr size_t PART_HEAD = 12 * SLOT_B;
constexpr size_t WS_PART_BYTES = H * PART_HEAD;
#define STAMP(i)

constexpr int Q_OFF = 0;
constexpr int RED_OFF = 98304;
constexpr int KP_OFF = RED_OFF + 32768;
constexpr int KP_STRIDE = 400;
constexpr int VT_OFF = KP_OFF + 8 * KP_STRIDE;
constexpr int CTL_OFF = VT_OFF + 192 * 16;
constexpr int SCR_OFF = CTL_OFF + 64;
constexpr int LDS_BYTES = SCR_OFF + 256;

__device__ __forceinline__ u32 as_u(float x) { return __builtin_bit_cast(u32, x); }
__device__ __forceinline__ float as_f(u32 x) { return __builtin_bit_cast(float, x); }
__device__ __forceinline__ f32x4 mfma32(v4u a, v4u b, f32x4 c) {
  return __builtin_amdgcn_mfma_f32_16x16x32_bf16(__builtin_bit_cast(s16x8, a), __builtin_bit_cast(s16x8, b), c, 0, 0, 0);
}
__device__ __forceinline__ f32x4 mfma16(v2u a, v2u b, f32x4 c) {
  return __builtin_amdgcn_mfma_f32_16x16x16bf16_1k(__builtin_bit_cast(s16x4, a), __builtin_bit_cast(s16x4, b), c, 0, 0, 0);
}
__device__ __forceinline__ u32 cvt_pk(float lo, float hi) {
  f32x2 v = {lo, hi};
  bf16x2 b = __builtin_convertvector(v, bf16x2);
  return __builtin_bit_cast(u32, b);
}
__device__ __forceinline__ float ror8(float x) {
  return as_f((u32)__builtin_amdgcn_update_dpp(0, (int)as_u(x), 0x128, 0xf, 0xf, true));
}
__device__ __forceinline__ float ror8_hi(float old, float x) {
  return as_f((u32)__builtin_amdgcn_update_dpp((int)as_u(old), (int)as_u(x), 0x128, 0xf, 0xc, false));
}
__device__ __forceinline__ void proj_pack(const f32x4 (&acc)[8], f32x4 (&z)[2]) {
#pragma unroll
  for (int e = 0; e < 2; ++e) {
#pragma unroll
    for (int reg = 0; reg < 4; ++reg) {
      auto s0 = __builtin_amdgcn_permlane32_swap(as_u(acc[4 * e + 0][reg]), as_u(acc[4 * e + 1][reg]), false, false);
      auto s1 = __builtin_amdgcn_permlane32_swap(as_u(acc[4 * e + 2][reg]), as_u(acc[4 * e + 3][reg]), false, false);
      const float x = as_f(s0[0]) + ror8(as_f(s0[1]));
      const float y = as_f(s1[0]) + ror8(as_f(s1[1]));
      z[e][reg] = ror8_hi(x, y);
    }
  }
}
typedef __amdgpu_buffer_rsrc_t rsrc_t;
template <int IMM> __device__ __forceinline__ void ld_nt(v4u& d, u32 voff, rsrc_t rs, u32 soff) {
  asm volatile("buffer_load_dwordx4 %0, %1, %2, %3 offen offset:%4 nt" : "=v"(d) : "v"(voff), "s"(rs), "s"(soff), "i"(IMM));
}
__device__ __forceinline__ void ld_plain(v4u& d, u32 voff, rsrc_t rs, u32 soff) {
  asm volatile("buffer_load_dwordx4 %0, %1, %2, %3 offen" : "=v"(d) : "v"(voff), "s"(rs), "s"(soff));
}
__device__ __forceinline__ void dma_nt(u32 lds_dst, u32 voff, rsrc_t rs, u32 soff) {
  asm volatile("s_mov_b32 m0, %0\n\ts_nop 0\n\tbuffer_load_dwordx4 %1, %2, %3 offen nt lds" ::"s"(lds_dst), "v"(voff), "s"(rs), "s"(soff) : "memory");
}
__device__ __forceinline__ u32 q_piece_off(int i, int g, int bb) {
  const int G = 4 * i + g, rho = (G * 43) >> 7;
  return (u32)(256 * G + 16 * (bb ^ rho));
}
template <int N> __device__ __forceinline__ void wait_vm() { asm volatile("s_waitcnt vmcnt(%0)" ::"i"(N) : "memory"); }

constexpr int NTHR = 512;
__device__ __forceinline__ u32 lds_ld(const LAS u32* p) { return (u32)__builtin_amdgcn_readfirstlane((int)__hip_atomic_load(p, __ATOMIC_RELAXED, __HIP_MEMORY_SCOPE_WORKGROUP)); }
__device__ __forceinline__ void lds_st(LAS u32* p, u32 v) { __hip_atomic_store(p, v, __ATOMIC_RELAXED, __HIP_MEMORY_SCOPE_WORKGROUP); }
__device__ __forceinline__ void lds_inc(LAS u32* p) {
  asm volatile("ds_add_u32 %0, %1" ::"v"((u32)(size_t)p), "v"(1u) : "memory");
}
#define CFENCE() asm volatile("" ::: "memory")
enum { C_X = 0, C_K = 1, C_V = 2 };

__global__ void __launch_bounds__(NTHR, 2)
linformer_fused(const uint16_t* __restrict__ Qp, const uint16_t* __restrict__ Kp, const uint16_t* __restrict__ Vp,
                const uint16_t* __restrict__ Ep, const uint16_t* __restrict__ Fp, uint16_t* __restrict__ Op, unsigned char* __restrict__ ws) {
  extern __shared__ __attribute__((aligned(16))) unsigned char smem[];
  LAS unsigned char* L = (LAS unsigned char*)smem;
  STAMP(0);
  const int b = blockIdx.x;
  const int h = (b & 7) * 4 + (b >> 6), j = (b >> 3) & 7;
  const int t = j >> 2, jj = j & 3;
  const int tid = threadIdx.x, lane = tid & 63, w = __builtin_amdgcn_readfirstlane(tid >> 6);
  const int g = lane >> 4, n = lane & 15;
  const u32 ldsb = (u32)(size_t)smem;
  LAS u32* ctl = (LAS u32*)(L + CTL_OFF);
  const u32 qtile = (u32)((h * S + 256 * j + 32 * w) * ROWB);
  const rsrc_t ro = __builtin_amdgcn_make_buffer_rsrc((void*)Op, 0, TENS_BYTES, 0x00020000);
  const rsrc_t rq = __builtin_amdgcn_make_buffer_rsrc((void*)Qp, 0, TENS_BYTES, 0x00020000);
  const rsrc_t rx = __builtin_amdgcn_make_buffer_rsrc((void*)(t ? Vp : Kp), 0, TENS_BYTES, 0x00020000);
  const rsrc_t rp = __builtin_amdgcn_make_buffer_rsrc((void*)(t ? Fp : Ep), 0, R * S * 2, 0x00020000);
  const rsrc_t rw = __builtin_amdgcn_make_buffer_rsrc((void*)ws, 0, (u32)(WS_PART + WS_PART_BYTES), 0x00020000);

  u32 ep;
  const int u0 = 24 * jj + 3 * w;
  const u32 voffE = (u32)((lane & 7) * (S * 2) + (16 * g + 8 * ((lane >> 3) & 1)) * 2);
  const u32 voffX = (u32)((16 * g + 8 * ((lane >> 3) & 1)) * ROWB + 16 * (lane & 7));
  v4u ef[3], x[3][8];
  u32 so[3];
#pragma unroll
  for (int i = 0; i < 3; ++i) so[i] = (u32)((h * S + ((u0 + i) & 31) * 64) * ROWB + 128 * ((u0 + i) >> 5));
#define LDX(i, k) { wait_vm<WX - 1>(); ld_nt<(k) * ROWB>(x[i][k], voffX, rx, so[i]); }
#define LDE(i) { wait_vm<WX - 1>(); ld_plain(ef[i], voffE, rp, (u32)(((u0 + i) & 31) * 128)); }
#define LDQA(i) { dma_nt(ldsb + Q_OFF + w * 12288 + (i) * 1024, q_piece_off((i), g, n), rq, qtile); }
#define LDQ(i) { wait_vm<WX - 1>(); dma_nt(ldsb + Q_OFF + w * 12288 + (i) * 1024, q_piece_off((i), g, n), rq, qtile); }
  ld_nt<0>(x[0][0], voffX, rx, so[0]); ld_nt<ROWB>(x[0][1], voffX, rx, so[0]);
  ld_plain(ef[0], voffE, rp, (u32)((u0 & 31) * 128));
  if (w == 0) {
    if (lane < 8) lds_st(ctl + lane, 0u);
    asm volatile("s_mov_b32 m0, %0\n\ts_nop 0\n\tbuffer_load_dword %1, %2, %3 offen nt lds" ::"s"(ldsb + SCR_OFF), "v"((u32)((lane & 1) * 98300)), "s"(ro), "s"(qtile) : "memory");
    asm volatile("s_mov_b32 m0, %0\n\ts_nop 0\n\tbuffer_load_dword %1, %2, %3 offen nt lds" ::"s"(ldsb + SCR_OFF), "v"((u32)((lane & 1) * 98300)), "s"(rq), "s"(qtile) : "memory");
  }
  const int slo = (24 * jj) >> 5;
  const int sA = u0 >> 5, sB = (u0 + 2) >> 5;
  LAS f32x4* red = (LAS f32x4*)(L + RED_OFF);
  {
    const f32x4 z0 = {0.f, 0.f, 0.f, 0.f};
    if (sA != slo) { red[(0 * 8 + w) * 128 + lane] = z0; red[(0 * 8 + w) * 128 + 64 + lane] = z0; }
    if (sB == slo) { red[(1 * 8 + w) * 128 + lane] = z0; red[(1 * 8 + w) * 128 + 64 + lane] = z0; }
  }
  asm volatile("s_waitcnt lgkmcnt(0)" ::: "memory");
  __builtin_amdgcn_s_barrier();
  CFENCE();
  LDX(0, 2) LDX(0, 3) LDX(0, 4) LDX(0, 5) LDX(0, 6) LDX(0, 7)
  LDE(1) LDX(1, 0) LDX(1, 1)

  f32x4 acc[8];
#define UNIT(i, ZERO)                                                                                                \
  _Pragma("unroll") for (int dd = 0; dd < 8; ++dd) {                                                                 \
    v4u f;                                                                                                           \
    _Pragma("unroll") for (int k = 0; k < 4; ++k)                                                                    \
        f[k] = __builtin_amdgcn_perm(x[i][2 * k + 1][dd >> 1], x[i][2 * k][dd >> 1], (dd & 1) ? 0x07060302u : 0x05040100u); \
    acc[dd] = mfma32(ef[i], f, ZERO ? f32x4{0.f, 0.f, 0.f, 0.f} : acc[dd]);                                          \
  }
#define FLUSH(strip)                                                                                                 \
  {                                                                                                                  \
    f32x4 z[2];                                                                                                      \
    proj_pack(acc, z);                                                                                               \
    const int sl = ((strip) != slo) ? 1 : 0;                                                                         \
    red[(sl * 8 + w) * 128 + lane] = z[0];                                                                           \
    red[(sl * 8 + w) * 128 + 64 + lane] = z[1];                                                                      \
  }
#define WAITX(i, N)                                                                                                  \
  asm volatile("s_waitcnt vmcnt(%9)"                                                                                 \
               : "+v"(x[i][0]), "+v"(x[i][1]), "+v"(x[i][2]), "+v"(x[i][3]), "+v"(x[i][4]), "+v"(x[i][5]), "+v"(x[i][6]), "+v"(x[i][7]), "+v"(ef[i]) \
               : "i"(N))
  WAITX(0, 3);
  LDX(1, 2)
  UNIT(0, true)
  const int s1 = (u0 + 1) >> 5;
  if (s1 != sA) {
    FLUSH(sA)
#pragma unroll
    for (int dd = 0; dd < 8; ++dd) acc[dd] = f32x4{0.f, 0.f, 0.f, 0.f};
  }
  LDX(1, 3) LDX(1, 4) LDX(1, 5) LDX(1, 6) LDX(1, 7)
  LDE(2) LDX(2, 0) LDX(2, 1)
  WAITX(1, 3);
  LDX(2, 2)
  UNIT(1, false)
  if (sB != s1) {
    FLUSH(s1)
#pragma unroll
    for (int dd = 0; dd < 8; ++dd) acc[dd] = f32x4{0.f, 0.f, 0.f, 0.f};
  }
  LDX(2, 3) LDX(2, 4) LDX(2, 5) LDX(2, 6) LDX(2, 7)
  wait_vm<WX - 1>();
  asm volatile("buffer_load_dword %0, off, %1, %2 sc1" : "=v"(ep) : "s"(rw), "s"((u32)(WS_EPOCH + h * 128)));
  LDQ(0) LDQ(1)
  STAMP(10);
  WAITX(2, 3);
  STAMP(1);
  LDQ(2)
  UNIT(2, false)
  FLUSH(sB)
  asm volatile("s_waitcnt lgkmcnt(0)" ::: "memory");
  if (lane == 0) lds_inc(ctl + C_X);
  asm volatile("s_waitcnt vmcnt(%1)" : "+v"(ep) : "i"(QAHEAD));
  const u32 tag = (u32)__builtin_amdgcn_readfirstlane(ep) + 1u;
  gu64* gran = (gu64*)(ws + WS_PART + h * PART_HEAD);

  auto publish = [&]() {
    CFENCE();
    STAMP(2);
    const int half = lane >> 5, which = (lane >> 4) & 1, it = 16 * w + (lane & 15);
    f32x4 v = red[(which * 8 + 4 * half + 0) * 128 + it];
#pragma unroll
    for (int k = 1; k < 4; ++k) v += red[(which * 8 + 4 * half + k) * 128 + it];
    f32x4 tot;
#pragma unroll
    for (int r = 0; r < 4; ++r) {
      auto sw = __builtin_amdgcn_permlane32_swap(as_u(v[r]), as_u(v[r]), false, false);
      tot[r] = as_f(sw[0]) + as_f(sw[1]);
    }
    const bool has_hi = (jj == 1 || jj == 2);
    const int slot = 6 * t + 2 * jj - (jj > 0 ? 1 : 0) + which;
    const u32 off = (half == 0 && (which == 0 || has_hi)) ? (u32)(WS_PART + h * PART_HEAD + slot * 2048 + it * 16) : 0xfffffff0u;
    __builtin_amdgcn_raw_buffer_store_b128(v4u{cvt_pk(tot[0], tot[1]), tag, cvt_pk(tot[2], tot[3]), tag}, rw, off, 0, PART_AUX);
    STAMP(3);
  };
  int published = 0;
  _Pragma("nounroll") for (int i = QAHEAD; i < 12; ++i) {
    published = __builtin_amdgcn_readfirstlane(published);
    if (!published && lds_ld(ctl + C_X) >= 8u) { publish(); published = 1; }
    wait_vm<WQ - 1>();
    dma_nt(ldsb + Q_OFF + w * 12288 + i * 1024, q_piece_off(i, g, n), rq, qtile);
  }
  published = __builtin_amdgcn_readfirstlane(published);
  if (!published) {
    _Pragma("nounroll") for (u32 spins = 0; spins < (1u << 22); ++spins) {
      if (lds_ld(ctl + C_X) >= 8u) break;
      __builtin_amdgcn_s_sleep(1);
    }
    publish();
  }
  constexpr int NG = F32PART ? 4 : 2;
  v4u gr[2][NG];
  const u32 po = (u32)(WS_PART + h * PART_HEAD + (2 * w) * SLOT_B) + lane * 16;
  if (w < 6) {
    STAMP(4);
#pragma unroll
    for (int p = 0; p < 2; ++p)
#pragma unroll
      for (int e = 0; e < NG; ++e) gr[p][e] = __builtin_amdgcn_raw_buffer_load_b128(rw, po + p * SLOT_B + e * 1024, 0, PART_LD_AUX);
    wait_vm<2 * NG>();
  } else {
    wait_vm<0>();
  }
  STAMP(12);
  v4u qf[12];
  {
    const LAS unsigned char* qrow = L + Q_OFF + w * 12288 + n * 768;
#pragma unroll
    for (int s = 0; s < 6; ++s) {
      qf[s] = *(const LAS v4u*)(qrow + 16 * ((4 * s + g) ^ n));
      qf[6 + s] = *(const LAS v4u*)(qrow + 16 * ((24 + 4 * s + g) ^ n));
    }
  }
  if (w < 6) {
    const int tt = w >= 3 ? 1 : 0, s = w - 3 * tt;
    const int ch = lane & 7, be = (lane >> 3) & 1, ga = (lane >> 4) & 1, et = lane >> 5;
    const int d0_ = 64 * s + 8 * ch + 2 * be + et;
    u32 dst[2];
#pragma unroll
    for (int e = 0; e < 2; ++e) {
      const int d = d0_ + 4 * e;
      dst[e] = ldsb + (tt == 0 ? KP_OFF + (4 * ga) * KP_STRIDE + 2 * d : VT_OFF + ((d & 7) >> 1) * 768 + (d >> 3) * 32 + ga * 16 + (d & 1) * 8);
    }
    asm volatile("" : "+v"(dst[0]), "+v"(dst[1]));
    u32 spins = 0;
    _Pragma("nounroll") for (; spins < (1u << 20); ++spins) {
      bool ok[2][NG], all = true;
#pragma unroll
      for (int p = 0; p < 2; ++p)
#pragma unroll
        for (int e = 0; e < NG; ++e) { ok[p][e] = (gr[p][e][1] == tag) & (gr[p][e][3] == tag); all &= ok[p][e]; }
      if (__all(all)) break;
      __builtin_amdgcn_s_sleep(RETRY_SLEEP);
#pragma unroll
      for (int p = 0; p < 2; ++p)
#pragma unroll
        for (int e = 0; e < NG; ++e)
          if (!ok[p][e]) gr[p][e] = __builtin_amdgcn_raw_buffer_load_b128(rw, po + p * SLOT_B + e * 1024, 0, PART_LD_AUX);
    }
    STAMP(5);
#pragma unroll
    for (int e = 0; e < 2; ++e) {
      const u32 a0 = gr[0][e][0], a1 = gr[0][e][2], b0 = gr[1][e][0], b1 = gr[1][e][2];
      const u32 v01 = cvt_pk(as_f(a0 << 16) + as_f(b0 << 16), as_f(a0 & 0xffff0000u) + as_f(b0 & 0xffff0000u));
      const u32 v23 = cvt_pk(as_f(a1 << 16) + as_f(b1 << 16), as_f(a1 & 0xffff0000u) + as_f(b1 & 0xffff0000u));
      if (tt == 0) {
        LAS uint16_t* kp = (LAS uint16_t*)(size_t)dst[e];
        kp[0] = (uint16_t)v01;
        kp[KP_STRIDE / 2] = (uint16_t)(v01 >> 16);
        kp[KP_STRIDE] = (uint16_t)v23;
        kp[3 * KP_STRIDE / 2] = (uint16_t)(v23 >> 16);
      } else {
        *(LAS v2u*)(size_t)dst[e] = v2u{v01, v23};
      }
    }
    asm volatile("s_waitcnt lgkmcnt(0)" ::: "memory");
    if (lane == 0) lds_inc(ctl + (w < 3 ? C_K : C_V));
    STAMP(6);
  }
  const LAS unsigned char* kfrag = L + KP_OFF + (n & 7) * KP_STRIDE + 16 * g;
  const LAS unsigned char* vfrag0 = L + VT_OFF + n * 32 + (g & 1) * 16;
  const LAS unsigned char* vfrag1 = L + VT_OFF + (n < 8 ? 16 + n : n - 8) * 32 + (g & 1) * 16;
  const LAS unsigned char* vfrag2 = L + VT_OFF + (8 + n) * 32 + (g & 1) * 16;
  const u32 ob = qtile + (4 * g) * 768 + 16 * n;
#pragma unroll
  for (int s = 0; s < 12; ++s) asm volatile("" : "+v"(qf[s]));
  _Pragma("nounroll") for (u32 spins = 0; spins < (1u << 24); ++spins) {
    if (lds_ld(ctl + C_K) >= 3u) break;
  }
  CFENCE();
  STAMP(7);
  f32x4 d0 = {0.f, 0.f, 0.f, 0.f}, d1 = {0.f, 0.f, 0.f, 0.f};
  {
    v4u F[6];
#pragma unroll
    for (int s = 0; s < 6; ++s) F[s] = *(const LAS v4u*)(kfrag + 64 * s);
#pragma unroll
    for (int s = 0; s < 6; ++s) {
      d0 = mfma32(F[s], qf[s], d0);
      d1 = mfma32(F[s], qf[6 + s], d1);
    }
  }
  const float sc = 0.07216878364870322f * 1.4426950408889634f;
  float sv[4];
#pragma unroll
  for (int r = 0; r < 4; ++r) sv[r] = (lane < 32 ? d0[r] : d1[r]);
  float m = fmaxf(fmaxf(sv[0], sv[1]), fmaxf(sv[2], sv[3]));
  {
    auto tt = __builtin_amdgcn_permlane16_swap(as_u(m), as_u(m), false, false);
    m = fmaxf(as_f(tt[0]), as_f(tt[1]));
  }
  float ps[4];
#pragma unroll
  for (int r = 0; r < 4; ++r) ps[r] = __builtin_amdgcn_exp2f((sv[r] - m) * sc);
  float sum = (ps[0] + ps[1]) + (ps[2] + ps[3]);
  {
    auto tt = __builtin_amdgcn_permlane16_swap(as_u(sum), as_u(sum), false, false);
    sum = as_f(tt[0]) + as_f(tt[1]);
  }
  const float inv = __builtin_amdgcn_rcpf(sum);
  const v2u pa = v2u{cvt_pk(ps[0] * inv, ps[1] * inv), cvt_pk(ps[2] * inv, ps[3] * inv)};
  const v2u pe = lane < 32 ? pa : v2u{0u, 0u}, po2 = lane < 32 ? v2u{0u, 0u} : pa;
  _Pragma("nounroll") for (u32 spins = 0; spins < (1u << 24); ++spins) {
    if (lds_ld(ctl + C_V) >= 3u) break;
  }
  CFENCE();
  if (j == 0 && tid == 0) __hip_atomic_store((GAS u32*)(ws + WS_EPOCH + h * 128), tag, __ATOMIC_RELAXED, __HIP_MEMORY_SCOPE_AGENT);
  {
    f32x4 o[8];
#pragma unroll
    for (int i = 0; i < 4; ++i) {
      const v4u v = *(const LAS v4u*)(vfrag0 + i * 768);
      o[2 * i] = mfma16(pe, v2u{v[0], v[1]}, f32x4{0.f, 0.f, 0.f, 0.f});
      o[2 * i + 1] = mfma16(pe, v2u{v[2], v[3]}, f32x4{0.f, 0.f, 0.f, 0.f});
    }
#pragma unroll
    for (int reg = 0; reg < 4; ++reg)
      __builtin_amdgcn_raw_buffer_store_b128(v4u{cvt_pk(o[0][reg], o[1][reg]), cvt_pk(o[2][reg], o[3][reg]), cvt_pk(o[4][reg], o[5][reg]), cvt_pk(o[6][reg], o[7][reg])}, ro, ob + reg * 768, 0, ST_AUX);
  }
  {
    f32x4 o[8];
#pragma unroll
    for (int i = 0; i < 4; ++i) {
      const v4u v = *(const LAS v4u*)(vfrag2 + i * 768);
      o[2 * i] = mfma16(po2, v2u{v[0], v[1]}, f32x4{0.f, 0.f, 0.f, 0.f});
      o[2 * i + 1] = mfma16(po2, v2u{v[2], v[3]}, f32x4{0.f, 0.f, 0.f, 0.f});
    }
#pragma unroll
    for (int reg = 0; reg < 4; ++reg)
      __builtin_amdgcn_raw_buffer_store_b128(v4u{cvt_pk(o[0][reg], o[1][reg]), cvt_pk(o[2][reg], o[3][reg]), cvt_pk(o[4][reg], o[5][reg]), cvt_pk(o[6][reg], o[7][reg])}, ro, ob + reg * 768 + 512, 0, ST_AUX);
  }
  {
    f32x4 o[8];
    const bool mine = (n < 8) == (lane < 32);
#pragma unroll
    for (int i = 0; i < 4; ++i) {
      const v4u v = *(const LAS v4u*)(vfrag1 + i * 768);
      o[2 * i] = mfma16(pa, mine ? v2u{v[0], v[1]} : v2u{0u, 0u}, f32x4{0.f, 0.f, 0.f, 0.f});
      o[2 * i + 1] = mfma16(pa, mine ? v2u{v[2], v[3]} : v2u{0u, 0u}, f32x4{0.f, 0.f, 0.f, 0.f});
    }
#pragma unroll
    for (int reg = 0; reg < 4; ++reg)
      __builtin_amdgcn_raw_buffer_store_b128(v4u{cvt_pk(o[0][reg], o[1][reg]), cvt_pk(o[2][reg], o[3][reg]), cvt_pk(o[4][reg], o[5][reg]), cvt_pk(o[6][reg], o[7][reg])}, ro, ob + reg * 768 + 256, 0, ST_AUX);
  }
  STAMP(8);
}

extern "C" void kernel_launch(void* const* d_in, const int* in_sizes, int n_in, void* d_out, int out_size, void* d_ws, size_t ws_size,
                              hipStream_t stream) {
  (void)hipFuncSetAttribute((const void*)linformer_fused, hipFuncAttributeMaxDynamicSharedMemorySize, LDS_BYTES);
  linformer_fused<<<NWG, NTHR, LDS_BYTES, stream>>>((const uint16_t*)d_in[0], (const uint16_t*)d_in[1], (const uint16_t*)d_in[2],
                                                   (const uint16_t*)d_in[3], (const uint16_t*)d_in[4], (uint16_t*)d_out, (unsigned char*)d_ws);
}
```

```cpp
#define LDPOL "nt"
#include <hip/hip_runtime.h>
#include <hip/hip_bf16.h>
#include <stdint.h>

#define GAS __attribute__((address_space(1)))
#define LAS __attribute__((address_space(3)))
typedef unsigned int u32;
typedef unsigned long long u64;
typedef u32 v4u __attribute__((ext_vector_type(4)));
typedef u32 v2u __attribute__((ext_vector_type(2)));
typedef float f32x4 __attribute__((ext_vector_type(4)));
typedef float f32x2 __attribute__((ext_vector_type(2)));
typedef short s16x8 __attribute__((ext_vector_type(8)));
typedef short s16x4 __attribute__((ext_vector_type(4)));
typedef __bf16 bf16x2 __attribute__((ext_vector_type(2)));
typedef GAS u64 gu64;
typedef __amdgpu_buffer_rsrc_t rsrc_t;

constexpr int H = 32, S = 2048, D = 192, R = 8;
constexpr int ROWB = D * 2;
constexpr u32 TENS_BYTES = (u32)H * S * ROWB;
constexpr int NWG = 256, NTHR = 512;
constexpr int WIN = 4;
constexpr int QAHEAD = 3;
constexpr int AUX_SC1 = 16, AUX_OUT = 18;
constexpr u32 RSRC3 = 0x00020000;

constexpr size_t WS_EPOCH = 0;
constexpr size_t WS_PART = 4096;
constexpr size_t PART_HEAD = 12 * 2048;

constexpr int Q_OFF = 0;
constexpr int RED_OFF = 98304;
constexpr int KP_OFF = RED_OFF + 32768;
constexpr int KP_STRIDE = 400;
constexpr int VT_OFF = KP_OFF + 8 * KP_STRIDE;
constexpr int CTL_OFF = VT_OFF + 192 * 16;
constexpr int LDS_BYTES = CTL_OFF + 64;
enum { C_X = 0, C_K = 1, C_V = 2 };

__device__ __forceinline__ u32 as_u(float x) { return __builtin_bit_cast(u32, x); }
__device__ __forceinline__ float as_f(u32 x) { return __builtin_bit_cast(float, x); }
__device__ __forceinline__ f32x4 mfma32(v4u a, v4u b, f32x4 c) {
  return __builtin_amdgcn_mfma_f32_16x16x32_bf16(__builtin_bit_cast(s16x8, a), __builtin_bit_cast(s16x8, b), c, 0, 0, 0);
}
__device__ __forceinline__ f32x4 mfma16(v2u a, v2u b) {
  return __builtin_amdgcn_mfma_f32_16x16x16bf16_1k(__builtin_bit_cast(s16x4, a), __builtin_bit_cast(s16x4, b), f32x4{0.f, 0.f, 0.f, 0.f}, 0, 0, 0);
}
__device__ __forceinline__ u32 cvt_pk(float lo, float hi) {
  const f32x2 v = {lo, hi};
  return __builtin_bit_cast(u32, __builtin_convertvector(v, bf16x2));
}
__device__ __forceinline__ float bf_lo(u32 x) { return as_f(x << 16); }
__device__ __forceinline__ float bf_hi(u32 x) { return as_f(x & 0xffff0000u); }
__device__ __forceinline__ float ror8(float x) {
  return as_f((u32)__builtin_amdgcn_update_dpp(0, (int)as_u(x), 0x128, 0xf, 0xf, true));
}
__device__ __forceinline__ float ror8_hi(float old, float x) {
  return as_f((u32)__builtin_amdgcn_update_dpp((int)as_u(old), (int)as_u(x), 0x128, 0xf, 0xc, false));
}

template <int IMM> __device__ __forceinline__ void ld_nt(v4u& d, u32 voff, rsrc_t rs, u32 soff) {
  asm volatile("buffer_load_dwordx4 %0, %1, %2, %3 offen offset:%4 " LDPOL : "=v"(d) : "v"(voff), "s"(rs), "s"(soff), "i"(IMM));
}
__device__ __forceinline__ void ld_plain(v4u& d, u32 voff, rsrc_t rs, u32 soff) {
  asm volatile("buffer_load_dwordx4 %0, %1, %2, %3 offen" : "=v"(d) : "v"(voff), "s"(rs), "s"(soff));
}
__device__ __forceinline__ void dma_nt(u32 lds_dst, u32 voff, rsrc_t rs, u32 soff) {
  asm volatile("s_mov_b32 m0, %0\n\ts_nop 0\n\tbuffer_load_dwordx4 %1, %2, %3 offen " LDPOL " lds" ::"s"(lds_dst), "v"(voff), "s"(rs), "s"(soff) : "memory");
}
template <int N> __device__ __forceinline__ void wait_vm() { asm volatile("s_waitcnt vmcnt(%0)" ::"i"(N) : "memory"); }
template <int N> __device__ __forceinline__ void wait_vm_plus(u32 sel) {
  asm volatile("s_waitcnt vmcnt(%1)\n\ts_cmp_lg_u32 %0, 0\n\ts_cbranch_scc1 1f\n\ts_waitcnt vmcnt(%2)\n1:" ::"s"(sel), "i"(N + 1), "i"(N) : "memory", "scc");
}
__device__ __forceinline__ void wait_lds() { asm volatile("s_waitcnt lgkmcnt(0)" ::: "memory"); }
#define CFENCE() asm volatile("" ::: "memory")

__device__ __forceinline__ u32 lds_ld(const LAS u32* p) {
  return (u32)__builtin_amdgcn_readfirstlane((int)__hip_atomic_load(p, __ATOMIC_RELAXED, __HIP_MEMORY_SCOPE_WORKGROUP));
}
__device__ __forceinline__ void lds_inc(LAS u32* p) {
  asm volatile("ds_add_u32 %0, %1" ::"v"((u32)(size_t)p), "v"(1u) : "memory");
}
__device__ __forceinline__ void lds_wait_ge(const LAS u32* p, u32 n) {
  _Pragma("nounroll") for (u32 spins = 0; spins < (1u << 24); ++spins)
    if (lds_ld(p) >= n) break;
  CFENCE();
}

__device__ __forceinline__ u32 q_piece_off(int i, int g, int n) {
  const int G = 4 * i + g, rho = (G * 43) >> 7;
  return (u32)(256 * G + 16 * (n ^ rho));
}

__device__ __forceinline__ void proj_pack(const f32x4 (&acc)[8], f32x4 (&z)[2]) {
#pragma unroll
  for (int e = 0; e < 2; ++e)
#pragma unroll
    for (int r = 0; r < 4; ++r) {
      auto s0 = __builtin_amdgcn_permlane32_swap(as_u(acc[4 * e + 0][r]), as_u(acc[4 * e + 1][r]), false, false);
      auto s1 = __builtin_amdgcn_permlane32_swap(as_u(acc[4 * e + 2][r]), as_u(acc[4 * e + 3][r]), false, false);
      const float x = as_f(s0[0]) + ror8(as_f(s0[1]));
      const float y = as_f(s1[0]) + ror8(as_f(s1[1]));
      z[e][r] = ror8_hi(x, y);
    }
}

__global__ void __launch_bounds__(NTHR, 2)
linformer_fused(const uint16_t* __restrict__ Qp, const uint16_t* __restrict__ Kp, const uint16_t* __restrict__ Vp,
                const uint16_t* __restrict__ Ep, const uint16_t* __restrict__ Fp, uint16_t* __restrict__ Op, unsigned char* __restrict__ ws) {
  extern __shared__ __attribute__((aligned(16))) unsigned char smem[];
  LAS unsigned char* L = (LAS unsigned char*)smem;
  const int b = blockIdx.x;
  const int h = (b & 7) * 4 + (b >> 6), j = (b >> 3) & 7;
  const int t = j >> 2, jj = j & 3;
  const int tid = threadIdx.x, lane = tid & 63, w = __builtin_amdgcn_readfirstlane(tid >> 6);
  const int g = lane >> 4, n = lane & 15;
  const u32 ldsb = (u32)(size_t)smem;
  LAS u32* ctl = (LAS u32*)(L + CTL_OFF);
  const u32 qtile = (u32)((h * S + 256 * j + 32 * w) * ROWB);
  const rsrc_t rx = __builtin_amdgcn_make_buffer_rsrc((void*)(t ? Vp : Kp), 0, TENS_BYTES, RSRC3);
  const rsrc_t rp = __builtin_amdgcn_make_buffer_rsrc((void*)(t ? Fp : Ep), 0, R * S * 2, RSRC3);
  const rsrc_t rq = __builtin_amdgcn_make_buffer_rsrc((void*)Qp, 0, TENS_BYTES, RSRC3);
  const rsrc_t ro = __builtin_amdgcn_make_buffer_rsrc((void*)Op, 0, TENS_BYTES, RSRC3);
  const rsrc_t rw = __builtin_amdgcn_make_buffer_rsrc((void*)ws, 0, 4096, RSRC3);

  const int u0 = 24 * jj + 3 * w;
  const u32 voffE = (u32)((lane & 7) * (S * 2) + (16 * g + 8 * ((lane >> 3) & 1)) * 2);
  const u32 voffX = (u32)((16 * g + 8 * ((lane >> 3) & 1)) * ROWB + 16 * (lane & 7));
  v4u ef[3], x[3][8];
  u32 so[3], ep;
#pragma unroll
  for (int i = 0; i < 3; ++i) so[i] = (u32)((h * S + ((u0 + i) & 31) * 64) * ROWB + 128 * ((u0 + i) >> 5));
#define LDX(i, k) { wait_vm<WIN - 1>(); ld_nt<(k) * ROWB>(x[i][k], voffX, rx, so[i]); }
#define LDXE(i, k) { wait_vm<WIN>(); ld_nt<(k) * ROWB>(x[i][k], voffX, rx, so[i]); }
#define LDE(i) ld_plain(ef[i], voffE, rp, (u32)(((u0 + i) & 31) * 128));
#define LDQ(i) { wait_vm<WIN - 1>(); dma_nt(ldsb + Q_OFF + w * 12288 + (i) * 1024, q_piece_off((i), g, n), rq, qtile); }
#define WAITX(i, N)                                                                                                  \
  asm volatile("s_waitcnt vmcnt(%9)"                                                                                 \
               : "+v"(x[i][0]), "+v"(x[i][1]), "+v"(x[i][2]), "+v"(x[i][3]), "+v"(x[i][4]), "+v"(x[i][5]), "+v"(x[i][6]), "+v"(x[i][7]), "+v"(ef[i]) \
               : "i"(N))
  ld_nt<0>(x[0][0], voffX, rx, so[0]);
  ld_nt<ROWB>(x[0][1], voffX, rx, so[0]);
  LDE(0)
  if (w == 0 && lane < 4) __hip_atomic_store(ctl + lane, 0u, __ATOMIC_RELAXED, __HIP_MEMORY_SCOPE_WORKGROUP);
  const int slo = (24 * jj) >> 5;
  const int sA = u0 >> 5, s1 = (u0 + 1) >> 5, sB = (u0 + 2) >> 5;
  LAS f32x4* red = (LAS f32x4*)(L + RED_OFF);
  {
    const f32x4 z0 = {0.f, 0.f, 0.f, 0.f};
    if (sA != slo) { red[(0 * 8 + w) * 128 + lane] = z0; red[(0 * 8 + w) * 128 + 64 + lane] = z0; }
    if (sB == slo) { red[(1 * 8 + w) * 128 + lane] = z0; red[(1 * 8 + w) * 128 + 64 + lane] = z0; }
  }
  wait_lds();
  __builtin_amdgcn_s_barrier();
  CFENCE();
  LDXE(0, 2) LDXE(0, 3) LDXE(0, 4) LDXE(0, 5) LDX(0, 6) LDX(0, 7)
  LDE(1) LDXE(1, 0) LDXE(1, 1)

  f32x4 acc[8];
#define UNIT(i, ZERO)                                                                                                \
  _Pragma("unroll") for (int dd = 0; dd < 8; ++dd) {                                                                 \
    v4u f;                                                                                                           \
    _Pragma("unroll") for (int k = 0; k < 4; ++k)                                                                    \
        f[k] = __builtin_amdgcn_perm(x[i][2 * k + 1][dd >> 1], x[i][2 * k][dd >> 1], (dd & 1) ? 0x07060302u : 0x05040100u); \
    acc[dd] = mfma32(ef[i], f, ZERO ? f32x4{0.f, 0.f, 0.f, 0.f} : acc[dd]);                                          \
  }
#define FLUSH(strip)                                                                                                 \
  {                                                                                                                  \
    f32x4 z[2];                                                                                                      \
    proj_pack(acc, z);                                                                                               \
    const int sl = ((strip) != slo) ? 1 : 0;                                                                         \
    red[(sl * 8 + w) * 128 + lane] = z[0];                                                                           \
    red[(sl * 8 + w) * 128 + 64 + lane] = z[1];                                                                      \
  }
#define CLEAR() _Pragma("unroll") for (int dd = 0; dd < 8; ++dd) acc[dd] = f32x4{0.f, 0.f, 0.f, 0.f};
  WAITX(0, 3);
  LDXE(1, 2)
  UNIT(0, true)
  if (s1 != sA) { FLUSH(sA) CLEAR() }
  LDXE(1, 3) LDX(1, 4) LDX(1, 5) LDX(1, 6) LDX(1, 7)
  LDE(2) LDXE(2, 0) LDXE(2, 1)
  WAITX(1, 3);
  LDXE(2, 2)
  UNIT(1, false)
  if (sB != s1) { FLUSH(s1) CLEAR() }
  LDXE(2, 3) LDX(2, 4) LDX(2, 5) LDX(2, 6) LDX(2, 7)
  wait_vm<WIN - 1>();
  asm volatile("buffer_load_dword %0, off, %1, %2 sc1" : "=v"(ep) : "s"(rw), "s"((u32)(WS_EPOCH + h * 128)));
  LDQ(0) LDQ(1)
  WAITX(2, 3);
  LDQ(2)
  UNIT(2, false)
  FLUSH(sB)
  wait_lds();
  if (lane == 0) lds_inc(ctl + C_X);
  asm volatile("s_waitcnt vmcnt(%1)" : "+v"(ep) : "i"(QAHEAD));
  const u32 tag = (u32)__builtin_amdgcn_readfirstlane(ep) + 1u;
  gu64* gran = (gu64*)(ws + WS_PART + h * PART_HEAD);

  auto publish = [&]() {
    CFENCE();
    const int half = lane >> 5, which = (lane >> 4) & 1, it = 16 * w + (lane & 15);
    f32x4 v = red[(which * 8 + 4 * half + 0) * 128 + it];
#pragma unroll
    for (int k = 1; k < 4; ++k) v += red[(which * 8 + 4 * half + k) * 128 + it];
    f32x4 tot;
#pragma unroll
    for (int r = 0; r < 4; ++r) {
      auto sw = __builtin_amdgcn_permlane32_swap(as_u(v[r]), as_u(v[r]), false, false);
      tot[r] = as_f(sw[0]) + as_f(sw[1]);
    }
    const int slot = 6 * t + 2 * jj - (jj > 0 ? 1 : 0) + which;
    const u32 pk = half ? cvt_pk(tot[2], tot[3]) : cvt_pk(tot[0], tot[1]);
    if (which == 0 || jj == 1 || jj == 2)
      __hip_atomic_store(gran + slot * 256 + half * 128 + it, ((u64)tag << 32) | pk, __ATOMIC_RELAXED, __HIP_MEMORY_SCOPE_AGENT);
  };
  int published = 0;
  _Pragma("nounroll") for (int i = QAHEAD; i < 12; ++i) {
    published = __builtin_amdgcn_readfirstlane(published);
    u32 cx;
    asm volatile("ds_read_b32 %0, %1" : "=v"(cx) : "v"(ldsb + CTL_OFF + 4 * C_X) : "memory");
    wait_vm_plus<WIN - 1>((u32)published);
    dma_nt(ldsb + Q_OFF + w * 12288 + i * 1024, q_piece_off(i, g, n), rq, qtile);
    asm volatile("s_waitcnt lgkmcnt(0)" : "+v"(cx)::"memory");
    if (!published && __builtin_amdgcn_readfirstlane((int)cx) >= 8) { publish(); published = 1; }
  }
  published = __builtin_amdgcn_readfirstlane(published);
  if (!published) {
    lds_wait_ge(ctl + C_X, 8u);
    publish();
  }
  wait_vm<0>();

  u64 gr[2][2][2];
  const gu64* gp = gran + (2 * w) * 256 + lane;
  if (w < 6) {
#pragma unroll
    for (int p = 0; p < 2; ++p)
#pragma unroll
      for (int hf = 0; hf < 2; ++hf)
#pragma unroll
        for (int e = 0; e < 2; ++e) gr[p][hf][e] = __hip_atomic_load(gp + p * 256 + hf * 128 + e * 64, __ATOMIC_RELAXED, __HIP_MEMORY_SCOPE_AGENT);
    CFENCE();
  }
  v4u qf[12];
  {
    const LAS unsigned char* qrow = L + Q_OFF + w * 12288 + n * 768;
#pragma unroll
    for (int s = 0; s < 6; ++s) {
      qf[s] = *(const LAS v4u*)(qrow + 16 * ((4 * s + g) ^ n));
      qf[6 + s] = *(const LAS v4u*)(qrow + 16 * ((24 + 4 * s + g) ^ n));
    }
  }
  if (w < 6) {
    const int tt = w >= 3 ? 1 : 0, s = w - 3 * tt;
    const int ch = lane & 7, be = (lane >> 3) & 1, ga = (lane >> 4) & 1, et = lane >> 5;
    u32 dst[2];
#pragma unroll
    for (int e = 0; e < 2; ++e) {
      const int d = 64 * s + 8 * ch + 4 * e + 2 * be + et;
      dst[e] = ldsb + (tt == 0 ? KP_OFF + (4 * ga) * KP_STRIDE + 2 * d : VT_OFF + ((d & 7) >> 1) * 768 + (d >> 3) * 32 + ga * 16 + (d & 1) * 8);
    }
    asm volatile("" : "+v"(dst[0]), "+v"(dst[1]));
    _Pragma("nounroll") for (u32 spins = 0; spins < (1u << 20); ++spins) {
      bool ok[2][2], all = true;
#pragma unroll
      for (int p = 0; p < 2; ++p)
#pragma unroll
        for (int e = 0; e < 2; ++e) { ok[p][e] = ((u32)(gr[p][0][e] >> 32) == tag) & ((u32)(gr[p][1][e] >> 32) == tag); all &= ok[p][e]; }
      if (__all(all)) break;
      __builtin_amdgcn_s_sleep(6);
#pragma unroll
      for (int p = 0; p < 2; ++p)
#pragma unroll
        for (int e = 0; e < 2; ++e)
          if (!ok[p][e]) {
            gr[p][0][e] = __hip_atomic_load(gp + p * 256 + e * 64, __ATOMIC_RELAXED, __HIP_MEMORY_SCOPE_AGENT);
            gr[p][1][e] = __hip_atomic_load(gp + p * 256 + 128 + e * 64, __ATOMIC_RELAXED, __HIP_MEMORY_SCOPE_AGENT);
          }
    }
#pragma unroll
    for (int e = 0; e < 2; ++e) {
      const u32 a0 = (u32)gr[0][0][e], a1 = (u32)gr[0][1][e], b0 = (u32)gr[1][0][e], b1 = (u32)gr[1][1][e];
      const u32 v01 = cvt_pk(bf_lo(a0) + bf_lo(b0), bf_hi(a0) + bf_hi(b0));
      const u32 v23 = cvt_pk(bf_lo(a1) + bf_lo(b1), bf_hi(a1) + bf_hi(b1));
      if (tt == 0) {
        LAS uint16_t* kp = (LAS uint16_t*)(size_t)dst[e];
        kp[0] = (uint16_t)v01;
        kp[KP_STRIDE / 2] = (uint16_t)(v01 >> 16);
        kp[KP_STRIDE] = (uint16_t)v23;
        kp[3 * KP_STRIDE / 2] = (uint16_t)(v23 >> 16);
      } else {
        *(LAS v2u*)(size_t)dst[e] = v2u{v01, v23};
      }
    }
    wait_lds();
    if (lane == 0) lds_inc(ctl + (w < 3 ? C_K : C_V));
  }

  const LAS unsigned char* kfrag = L + KP_OFF + (n & 7) * KP_STRIDE + 16 * g;
  const LAS unsigned char* vfragA = L + VT_OFF + n * 32 + (g & 1) * 16;
  const LAS unsigned char* vfragB = L + VT_OFF + (n < 8 ? 16 + n : n - 8) * 32 + (g & 1) * 16;
  const LAS unsigned char* vfragC = L + VT_OFF + (8 + n) * 32 + (g & 1) * 16;
  const u32 ob = qtile + (4 * g) * 768 + 16 * n;
#pragma unroll
  for (int s = 0; s < 12; ++s) asm volatile("" : "+v"(qf[s]));
  lds_wait_ge(ctl + C_K, 3u);
  f32x4 d0 = {0.f, 0.f, 0.f, 0.f}, d1 = {0.f, 0.f, 0.f, 0.f};
  {
    v4u F[6];
#pragma unroll
    for (int s = 0; s < 6; ++s) F[s] = *(const LAS v4u*)(kfrag + 64 * s);
#pragma unroll
    for (int s = 0; s < 6; ++s) {
      d0 = mfma32(F[s], qf[s], d0);
      d1 = mfma32(F[s], qf[6 + s], d1);
    }
  }
  const float sc = 0.07216878364870322f * 1.4426950408889634f;
  float sv[4];
#pragma unroll
  for (int r = 0; r < 4; ++r) sv[r] = (lane < 32 ? d0[r] : d1[r]);
  float m = fmaxf(fmaxf(sv[0], sv[1]), fmaxf(sv[2], sv[3]));
  {
    auto x2 = __builtin_amdgcn_permlane16_swap(as_u(m), as_u(m), false, false);
    m = fmaxf(as_f(x2[0]), as_f(x2[1]));
  }
  float ps[4];
#pragma unroll
  for (int r = 0; r < 4; ++r) ps[r] = __builtin_amdgcn_exp2f((sv[r] - m) * sc);
  float sum = (ps[0] + ps[1]) + (ps[2] + ps[3]);
  {
    auto x2 = __builtin_amdgcn_permlane16_swap(as_u(sum), as_u(sum), false, false);
    sum = as_f(x2[0]) + as_f(x2[1]);
  }
  const float inv = __builtin_amdgcn_rcpf(sum);
  const v2u pa = v2u{cvt_pk(ps[0] * inv, ps[1] * inv), cvt_pk(ps[2] * inv, ps[3] * inv)};
  const v2u pe = lane < 32 ? pa : v2u{0u, 0u}, po = lane < 32 ? v2u{0u, 0u} : pa;
  lds_wait_ge(ctl + C_V, 3u);
  if (j == 0 && tid == 0) __hip_atomic_store((GAS u32*)(ws + WS_EPOCH + h * 128), tag, __ATOMIC_RELAXED, __HIP_MEMORY_SCOPE_AGENT);
#define PV_GROUP(A, FRAG, MASK, OFFS)                                                                                \
  {                                                                                                                  \
    f32x4 o[8];                                                                                                      \
    _Pragma("unroll") for (int i = 0; i < 4; ++i) {                                                                  \
      const v4u v = *(const LAS v4u*)((FRAG) + i * 768);                                                             \
      o[2 * i] = mfma16(A, (MASK) ? v2u{v[0], v[1]} : v2u{0u, 0u});                                                  \
      o[2 * i + 1] = mfma16(A, (MASK) ? v2u{v[2], v[3]} : v2u{0u, 0u});                                              \
    }                                                                                                                \
    _Pragma("unroll") for (int r = 0; r < 4; ++r)                                                                    \
      __builtin_amdgcn_raw_buffer_store_b128(v4u{cvt_pk(o[0][r], o[1][r]), cvt_pk(o[2][r], o[3][r]), cvt_pk(o[4][r], o[5][r]), cvt_pk(o[6][r], o[7][r])}, \
                                             ro, ob + r * 768 + (OFFS), 0, AUX_OUT);                                  \
  }
  PV_GROUP(pe, vfragA, true, 0)
  PV_GROUP(po, vfragC, true, 512)
  const bool mine = (n < 8) == (lane < 32);
  PV_GROUP(pa, vfragB, mine, 256)
}

extern "C" void kernel_launch(void* const* d_in, const int* in_sizes, int n_in, void* d_out, int out_size, void* d_ws, size_t ws_size,
                              hipStream_t stream) {
  (void)in_sizes; (void)n_in; (void)out_size; (void)ws_size;
  (void)hipFuncSetAttribute((const void*)linformer_fused, hipFuncAttributeMaxDynamicSharedMemorySize, LDS_BYTES);
  linformer_fused<<<NWG, NTHR, LDS_BYTES, stream>>>((const uint16_t*)d_in[0], (const uint16_t*)d_in[1], (const uint16_t*)d_in[2],
                                                    (const uint16_t*)d_in[3], (const uint16_t*)d_in[4], (uint16_t*)d_out, (unsigned char*)d_ws);
}
```

```cpp
#include <hip/hip_runtime.h>
#include <hip/hip_bf16.h>
#include <stdint.h>

#define GAS __attribute__((address_space(1)))
#define LAS __attribute__((address_space(3)))
typedef unsigned int u32;
typedef unsigned long long u64;
typedef u32 v4u __attribute__((ext_vector_type(4)));
typedef u32 v2u __attribute__((ext_vector_type(2)));
typedef float f32x4 __attribute__((ext_vector_type(4)));
typedef float f32x2 __attribute__((ext_vector_type(2)));
typedef short s16x8 __attribute__((ext_vector_type(8)));
typedef short s16x4 __attribute__((ext_vector_type(4)));
typedef __bf16 bf16x2 __attribute__((ext_vector_type(2)));
typedef GAS u64 gu64;
typedef __amdgpu_buffer_rsrc_t rsrc_t;

constexpr int H = 32, S = 2048, D = 192, R = 8;
constexpr int ROWB = D * 2;
constexpr u32 TENS_BYTES = (u32)H * S * ROWB;
constexpr int NWG = 256, NTHR = 512;
constexpr int WIN = 4;
constexpr int QAHEAD = 3;
constexpr int AUX_OUT = 17;
constexpr u32 RSRC3 = 0x00020000;

constexpr size_t WS_EPOCH = 0;
constexpr size_t WS_PART = 4096;
constexpr size_t PART_HEAD = 12 * 2048;

constexpr int Q_OFF = 0;
constexpr int RED_OFF = 98304;
constexpr int KP_OFF = RED_OFF + 32768;
constexpr int KP_STRIDE = 400;
constexpr int VT_OFF = KP_OFF + 8 * KP_STRIDE;
constexpr int CTL_OFF = VT_OFF + 192 * 16;
constexpr int LDS_BYTES = CTL_OFF + 64;
enum { C_X = 0, C_K = 1, C_V = 2 };

__device__ __forceinline__ u32 as_u(float x) { return __builtin_bit_cast(u32, x); }
__device__ __forceinline__ float as_f(u32 x) { return __builtin_bit_cast(float, x); }
__device__ __forceinline__ f32x4 mfma32(v4u a, v4u b, f32x4 c) {
  return __builtin_amdgcn_mfma_f32_16x16x32_bf16(__builtin_bit_cast(s16x8, a), __builtin_bit_cast(s16x8, b), c, 0, 0, 0);
}
__device__ __forceinline__ f32x4 mfma16(v2u a, v2u b) {
  return __builtin_amdgcn_mfma_f32_16x16x16bf16_1k(__builtin_bit_cast(s16x4, a), __builtin_bit_cast(s16x4, b), f32x4{0.f, 0.f, 0.f, 0.f}, 0, 0, 0);
}
__device__ __forceinline__ u32 cvt_pk(float lo, float hi) {
  const f32x2 v = {lo, hi};
  return __builtin_bit_cast(u32, __builtin_convertvector(v, bf16x2));
}
__device__ __forceinline__ float bf_lo(u32 x) { return as_f(x << 16); }
__device__ __forceinline__ float bf_hi(u32 x) { return as_f(x & 0xffff0000u); }
__device__ __forceinline__ float ror8(float x) {
  return as_f((u32)__builtin_amdgcn_update_dpp(0, (int)as_u(x), 0x128, 0xf, 0xf, true));
}
__device__ __forceinline__ float ror8_hi(float old, float x) {
  return as_f((u32)__builtin_amdgcn_update_dpp((int)as_u(old), (int)as_u(x), 0x128, 0xf, 0xc, false));
}

template <int IMM> __device__ __forceinline__ void ld_nt(v4u& d, u32 voff, rsrc_t rs, u32 soff) {
  asm volatile("buffer_load_dwordx4 %0, %1, %2, %3 offen offset:%4 nt" : "=v"(d) : "v"(voff), "s"(rs), "s"(soff), "i"(IMM));
}
__device__ __forceinline__ void ld_plain(v4u& d, u32 voff, rsrc_t rs, u32 soff) {
  asm volatile("buffer_load_dwordx4 %0, %1, %2, %3 offen" : "=v"(d) : "v"(voff), "s"(rs), "s"(soff));
}
__device__ __forceinline__ void dma_nt(u32 lds_dst, u32 voff, rsrc_t rs, u32 soff) {
  asm volatile("s_mov_b32 m0, %0\n\ts_nop 0\n\tbuffer_load_dwordx4 %1, %2, %3 offen nt lds" ::"s"(lds_dst), "v"(voff), "s"(rs), "s"(soff) : "memory");
}
template <int N> __device__ __forceinline__ void wait_vm() { asm volatile("s_waitcnt vmcnt(%0)" ::"i"(N) : "memory"); }
template <int N> __device__ __forceinline__ void wait_vm_plus(u32 sel) {
  asm volatile("s_waitcnt vmcnt(%1)\n\ts_cmp_lg_u32 %0, 0\n\ts_cbranch_scc1 1f\n\ts_waitcnt vmcnt(%2)\n1:" ::"s"(sel), "i"(N + 1), "i"(N) : "memory", "scc");
}
__device__ __forceinline__ void wait_lds() { asm volatile("s_waitcnt lgkmcnt(0)" ::: "memory"); }
#define CFENCE() asm volatile("" ::: "memory")

__device__ __forceinline__ u32 lds_ld(const LAS u32* p) {
  return (u32)__builtin_amdgcn_readfirstlane((int)__hip_atomic_load(p, __ATOMIC_RELAXED, __HIP_MEMORY_SCOPE_WORKGROUP));
}
__device__ __forceinline__ void lds_inc(LAS u32* p) {
  asm volatile("ds_add_u32 %0, %1" ::"v"((u32)(size_t)p), "v"(1u) : "memory");
}
__device__ __forceinline__ void lds_wait_ge(const LAS u32* p, u32 n) {
  _Pragma("nounroll") for (u32 spins = 0; spins < (1u << 24); ++spins)
    if (lds_ld(p) >= n) break;
  CFENCE();
}

__device__ __forceinline__ u32 q_piece_off(int i, int g, int n) {
  const int G = 4 * i + g, rho = (G * 43) >> 7;
  return (u32)(256 * G + 16 * (n ^ rho));
}

__device__ __forceinline__ void proj_pack(const f32x4 (&acc)[8], f32x4 (&z)[2]) {
#pragma unroll
  for (int e = 0; e < 2; ++e)
#pragma unroll
    for (int r = 0; r < 4; ++r) {
      auto s0 = __builtin_amdgcn_permlane32_swap(as_u(acc[4 * e + 0][r]), as_u(acc[4 * e + 1][r]), false, false);
      auto s1 = __builtin_amdgcn_permlane32_swap(as_u(acc[4 * e + 2][r]), as_u(acc[4 * e + 3][r]), false, false);
      const float x = as_f(s0[0]) + ror8(as_f(s0[1]));
      const float y = as_f(s1[0]) + ror8(as_f(s1[1]));
      z[e][r] = ror8_hi(x, y);
    }
}

__global__ void __launch_bounds__(NTHR, 2)
linformer_fused(const uint16_t* __restrict__ Qp, const uint16_t* __restrict__ Kp, const uint16_t* __restrict__ Vp,
                const uint16_t* __restrict__ Ep, const uint16_t* __restrict__ Fp, uint16_t* __restrict__ Op, unsigned char* __restrict__ ws) {
  extern __shared__ __attribute__((aligned(16))) unsigned char smem[];
  LAS unsigned char* L = (LAS unsigned char*)smem;
  const int b = blockIdx.x;
  const int h = (b & 7) * 4 + (b >> 6), j = (b >> 3) & 7;
  const int t = j >> 2, jj = j & 3;
  const int tid = threadIdx.x, lane = tid & 63, w = __builtin_amdgcn_readfirstlane(tid >> 6);
  const int g = lane >> 4, n = lane & 15;
  const u32 ldsb = (u32)(size_t)smem;
  LAS u32* ctl = (LAS u32*)(L + CTL_OFF);
  const u32 qtile = (u32)((h * S + 256 * j + 32 * w) * ROWB);
  const rsrc_t rx = __builtin_amdgcn_make_buffer_rsrc((void*)(t ? Vp : Kp), 0, TENS_BYTES, RSRC3);
  const rsrc_t rp = __builtin_amdgcn_make_buffer_rsrc((void*)(t ? Fp : Ep), 0, R * S * 2, RSRC3);
  const rsrc_t rq = __builtin_amdgcn_make_buffer_rsrc((void*)Qp, 0, TENS_BYTES, RSRC3);
  const rsrc_t ro = __builtin_amdgcn_make_buffer_rsrc((void*)Op, 0, TENS_BYTES, RSRC3);
  const rsrc_t rw = __builtin_amdgcn_make_buffer_rsrc((void*)ws, 0, 4096, RSRC3);

  const int u0 = 24 * jj + 3 * w;
  const u32 voffE = (u32)((lane & 7) * (S * 2) + (16 * g + 8 * ((lane >> 3) & 1)) * 2);
  const u32 voffX = (u32)((16 * g + 8 * ((lane >> 3) & 1)) * ROWB + 16 * (lane & 7));
  v4u ef[3], x[3][8];
  u32 so[3], ep;
#pragma unroll
  for (int i = 0; i < 3; ++i) so[i] = (u32)((h * S + ((u0 + i) & 31) * 64) * ROWB + 128 * ((u0 + i) >> 5));
#define LDX(i, k) { wait_vm<WIN - 1>(); ld_nt<(k) * ROWB>(x[i][k], voffX, rx, so[i]); }
#define LDXE(i, k) { wait_vm<WIN>(); ld_nt<(k) * ROWB>(x[i][k], voffX, rx, so[i]); }
#define LDE(i) ld_plain(ef[i], voffE, rp, (u32)(((u0 + i) & 31) * 128));
#define LDQ(i) { wait_vm<WIN - 1>(); dma_nt(ldsb + Q_OFF + w * 12288 + (i) * 1024, q_piece_off((i), g, n), rq, qtile); }
#define WAITX(i, N)                                                                                                  \
  asm volatile("s_waitcnt vmcnt(%9)"                                                                                 \
               : "+v"(x[i][0]), "+v"(x[i][1]), "+v"(x[i][2]), "+v"(x[i][3]), "+v"(x[i][4]), "+v"(x[i][5]), "+v"(x[i][6]), "+v"(x[i][7]), "+v"(ef[i]) \
               : "i"(N))
  ld_nt<0>(x[0][0], voffX, rx, so[0]);
  ld_nt<ROWB>(x[0][1], voffX, rx, so[0]);
  LDE(0)
  if (w == 0 && lane < 4) __hip_atomic_store(ctl + lane, 0u, __ATOMIC_RELAXED, __HIP_MEMORY_SCOPE_WORKGROUP);
  const int slo = (24 * jj) >> 5;
  const int sA = u0 >> 5, s1 = (u0 + 1) >> 5, sB = (u0 + 2) >> 5;
  LAS f32x4* red = (LAS f32x4*)(L + RED_OFF);
  {
    const f32x4 z0 = {0.f, 0.f, 0.f, 0.f};
    if (sA != slo) { red[(0 * 8 + w) * 128 + lane] = z0; red[(0 * 8 + w) * 128 + 64 + lane] = z0; }
    if (sB == slo) { red[(1 * 8 + w) * 128 + lane] = z0; red[(1 * 8 + w) * 128 + 64 + lane] = z0; }
  }
  wait_lds();
  __builtin_amdgcn_s_barrier();
  CFENCE();
  LDXE(0, 2) LDXE(0, 3) LDXE(0, 4) LDXE(0, 5) LDX(0, 6) LDX(0, 7)
  LDE(1) LDXE(1, 0) LDXE(1, 1)

  f32x4 acc[8];
#define UNIT(i, ZERO)                                                                                                \
  _Pragma("unroll") for (int dd = 0; dd < 8; ++dd) {                                                                 \
    v4u f;                                                                                                           \
    _Pragma("unroll") for (int k = 0; k < 4; ++k)                                                                    \
        f[k] = __builtin_amdgcn_perm(x[i][2 * k + 1][dd >> 1], x[i][2 * k][dd >> 1], (dd & 1) ? 0x07060302u : 0x05040100u); \
    acc[dd] = mfma32(ef[i], f, ZERO ? f32x4{0.f, 0.f, 0.f, 0.f} : acc[dd]);                                          \
  }
#define FLUSH(strip)                                                                                                 \
  {                                                                                                                  \
    f32x4 z[2];                                                                                                      \
    proj_pack(acc, z);                                                                                               \
    const int sl = ((strip) != slo) ? 1 : 0;                                                                         \
    red[(sl * 8 + w) * 128 + lane] = z[0];                                                                           \
    red[(sl * 8 + w) * 128 + 64 + lane] = z[1];                                                                      \
  }
#define CLEAR() _Pragma("unroll") for (int dd = 0; dd < 8; ++dd) acc[dd] = f32x4{0.f, 0.f, 0.f, 0.f};
  WAITX(0, 3);
  LDXE(1, 2)
  UNIT(0, true)
  if (s1 != sA) { FLUSH(sA) CLEAR() }
  LDXE(1, 3) LDX(1, 4) LDX(1, 5) LDX(1, 6) LDX(1, 7)
  LDE(2) LDXE(2, 0) LDXE(2, 1)
  WAITX(1, 3);
  LDXE(2, 2)
  UNIT(1, false)
  if (sB != s1) { FLUSH(s1) CLEAR() }
  LDXE(2, 3) LDX(2, 4) LDX(2, 5) LDX(2, 6) LDX(2, 7)
  wait_vm<WIN - 1>();
  asm volatile("buffer_load_dword %0, off, %1, %2 sc1" : "=v"(ep) : "s"(rw), "s"((u32)(WS_EPOCH + h * 128)));
  LDQ(0) LDQ(1)
  WAITX(2, 3);
  LDQ(2)
  UNIT(2, false)
  FLUSH(sB)
  wait_lds();
  if (lane == 0) lds_inc(ctl + C_X);
  asm volatile("s_waitcnt vmcnt(%1)" : "+v"(ep) : "i"(QAHEAD));
  const u32 tag = (u32)__builtin_amdgcn_readfirstlane(ep) + 1u;
  gu64* gran = (gu64*)(ws + WS_PART + h * PART_HEAD);

  auto publish = [&]() {
    CFENCE();
    const int half = lane >> 5, which = (lane >> 4) & 1, it = 16 * w + (lane & 15);
    f32x4 v = red[(which * 8 + 4 * half + 0) * 128 + it];
#pragma unroll
    for (int k = 1; k < 4; ++k) v += red[(which * 8 + 4 * half + k) * 128 + it];
    f32x4 tot;
#pragma unroll
    for (int r = 0; r < 4; ++r) {
      auto sw = __builtin_amdgcn_permlane32_swap(as_u(v[r]), as_u(v[r]), false, false);
      tot[r] = as_f(sw[0]) + as_f(sw[1]);
    }
    const int slot = 6 * t + 2 * jj - (jj > 0 ? 1 : 0) + which;
    const u32 pk = half ? cvt_pk(tot[2], tot[3]) : cvt_pk(tot[0], tot[1]);
    if (which == 0 || jj == 1 || jj == 2)
      __hip_atomic_store(gran + slot * 256 + half * 128 + it, ((u64)tag << 32) | pk, __ATOMIC_RELAXED, __HIP_MEMORY_SCOPE_AGENT);
  };
  int published = 0;
  _Pragma("nounroll") for (int i = QAHEAD; i < 12; ++i) {
    published = __builtin_amdgcn_readfirstlane(published);
    u32 cx;
    asm volatile("ds_read_b32 %0, %1" : "=v"(cx) : "v"(ldsb + CTL_OFF + 4 * C_X) : "memory");
    wait_vm_plus<WIN - 1>((u32)published);
    dma_nt(ldsb + Q_OFF + w * 12288 + i * 1024, q_piece_off(i, g, n), rq, qtile);
    asm volatile("s_waitcnt lgkmcnt(0)" : "+v"(cx)::"memory");
    if (!published && __builtin_amdgcn_readfirstlane((int)cx) >= 8) { publish(); published = 1; }
  }
  published = __builtin_amdgcn_readfirstlane(published);
  if (!published) {
    lds_wait_ge(ctl + C_X, 8u);
    publish();
  }
  wait_vm<0>();

  u64 gr[2][2][2];
  const gu64* gp = gran + (2 * w) * 256 + lane;
  if (w < 6) {
#pragma unroll
    for (int p = 0; p < 2; ++p)
#pragma unroll
      for (int hf = 0; hf < 2; ++hf)
#pragma unroll
        for (int e = 0; e < 2; ++e) gr[p][hf][e] = __hip_atomic_load(gp + p * 256 + hf * 128 + e * 64, __ATOMIC_RELAXED, __HIP_MEMORY_SCOPE_AGENT);
    CFENCE();
  }
  v4u qf[12];
  {
    const LAS unsigned char* qrow = L + Q_OFF + w * 12288 + n * 768;
#pragma unroll
    for (int s = 0; s < 6; ++s) {
      qf[s] = *(const LAS v4u*)(qrow + 16 * ((4 * s + g) ^ n));
      qf[6 + s] = *(const LAS v4u*)(qrow + 16 * ((24 + 4 * s + g) ^ n));
    }
  }
  if (w < 6) {
    const int tt = w >= 3 ? 1 : 0, s = w - 3 * tt;
    const int ch = lane & 7, be = (lane >> 3) & 1, ga = (lane >> 4) & 1, et = lane >> 5;
    u32 dst[2];
#pragma unroll
    for (int e = 0; e < 2; ++e) {
      const int d = 64 * s + 8 * ch + 4 * e + 2 * be + et;
      dst[e] = ldsb + (tt == 0 ? KP_OFF + (4 * ga) * KP_STRIDE + 2 * d : VT_OFF + ((d & 7) >> 1) * 768 + (d >> 3) * 32 + ga * 16 + (d & 1) * 8);
    }
    asm volatile("" : "+v"(dst[0]), "+v"(dst[1]));
    _Pragma("nounroll") for (u32 spins = 0; spins < (1u << 20); ++spins) {
      bool ok[2][2], all = true;
#pragma unroll
      for (int p = 0; p < 2; ++p)
#pragma unroll
        for (int e = 0; e < 2; ++e) { ok[p][e] = ((u32)(gr[p][0][e] >> 32) == tag) & ((u32)(gr[p][1][e] >> 32) == tag); all &= ok[p][e]; }
      if (__all(all)) break;
      __builtin_amdgcn_s_sleep(6);
#pragma unroll
      for (int p = 0; p < 2; ++p)
#pragma unroll
        for (int e = 0; e < 2; ++e)
          if (!ok[p][e]) {
            gr[p][0][e] = __hip_atomic_load(gp + p * 256 + e * 64, __ATOMIC_RELAXED, __HIP_MEMORY_SCOPE_AGENT);
            gr[p][1][e] = __hip_atomic_load(gp + p * 256 + 128 + e * 64, __ATOMIC_RELAXED, __HIP_MEMORY_SCOPE_AGENT);
          }
    }
#pragma unroll
    for (int e = 0; e < 2; ++e) {
      const u32 a0 = (u32)gr[0][0][e], a1 = (u32)gr[0][1][e], b0 = (u32)gr[1][0][e], b1 = (u32)gr[1][1][e];
      const u32 v01 = cvt_pk(bf_lo(a0) + bf_lo(b0), bf_hi(a0) + bf_hi(b0));
      const u32 v23 = cvt_pk(bf_lo(a1) + bf_lo(b1), bf_hi(a1) + bf_hi(b1));
      if (tt == 0) {
        LAS uint16_t* kp = (LAS uint16_t*)(size_t)dst[e];
        kp[0] = (uint16_t)v01;
        kp[KP_STRIDE / 2] = (uint16_t)(v01 >> 16);
        kp[KP_STRIDE] = (uint16_t)v23;
        kp[3 * KP_STRIDE / 2] = (uint16_t)(v23 >> 16);
      } else {
        *(LAS v2u*)(size_t)dst[e] = v2u{v01, v23};
      }
    }
    wait_lds();
    if (lane == 0) lds_inc(ctl + (w < 3 ? C_K : C_V));
  }

  const LAS unsigned char* kfrag = L + KP_OFF + (n & 7) * KP_STRIDE + 16 * g;
  const LAS unsigned char* vfragA = L + VT_OFF + n * 32 + (g & 1) * 16;
  const LAS unsigned char* vfragB = L + VT_OFF + (n < 8 ? 16 + n : n - 8) * 32 + (g & 1) * 16;
  const LAS unsigned char* vfragC = L + VT_OFF + (8 + n) * 32 + (g & 1) * 16;
  const u32 ob = qtile + (4 * g) * 768 + 16 * n;
#pragma unroll
  for (int s = 0; s < 12; ++s) asm volatile("" : "+v"(qf[s]));
  lds_wait_ge(ctl + C_K, 3u);
  f32x4 d0 = {0.f, 0.f, 0.f, 0.f}, d1 = {0.f, 0.f, 0.f, 0.f};
  {
    v4u F[6];
#pragma unroll
    for (int s = 0; s < 6; ++s) F[s] = *(const LAS v4u*)(kfrag + 64 * s);
#pragma unroll
    for (int s = 0; s < 6; ++s) {
      d0 = mfma32(F[s], qf[s], d0);
      d1 = mfma32(F[s], qf[6 + s], d1);
    }
  }
  lds_wait_ge(ctl + C_V, 3u);
  v4u vb[3][4];
#pragma unroll
  for (int i = 0; i < 4; ++i) { vb[0][i] = *(const LAS v4u*)(vfragA + i * 768); vb[1][i] = *(const LAS v4u*)(vfragC + i * 768); vb[2][i] = *(const LAS v4u*)(vfragB + i * 768); }
  if (j == 0 && tid == 0) __hip_atomic_store((GAS u32*)(ws + WS_EPOCH + h * 128), tag, __ATOMIC_RELAXED, __HIP_MEMORY_SCOPE_AGENT);
  const float sc = 0.07216878364870322f * 1.4426950408889634f;
  float sv[4];
#pragma unroll
  for (int r = 0; r < 4; ++r) sv[r] = (lane < 32 ? d0[r] : d1[r]);
  float m = fmaxf(fmaxf(sv[0], sv[1]), fmaxf(sv[2], sv[3]));
  {
    auto x2 = __builtin_amdgcn_permlane16_swap(as_u(m), as_u(m), false, false);
    m = fmaxf(as_f(x2[0]), as_f(x2[1]));
  }
  float ps[4];
#pragma unroll
  for (int r = 0; r < 4; ++r) ps[r] = __builtin_amdgcn_exp2f((sv[r] - m) * sc);
  float sum = (ps[0] + ps[1]) + (ps[2] + ps[3]);
  {
    auto x2 = __builtin_amdgcn_permlane16_swap(as_u(sum), as_u(sum), false, false);
    sum = as_f(x2[0]) + as_f(x2[1]);
  }
  const float inv = __builtin_amdgcn_rcpf(sum);
  const v2u pa = v2u{cvt_pk(ps[0] * inv, ps[1] * inv), cvt_pk(ps[2] * inv, ps[3] * inv)};
  const v2u pe = lane < 32 ? pa : v2u{0u, 0u}, po = lane < 32 ? v2u{0u, 0u} : pa;
#define PV_GROUP(A, VB, MASK, OFFS)                                                                                  \
  {                                                                                                                  \
    f32x4 o[8];                                                                                                      \
    _Pragma("unroll") for (int i = 0; i < 4; ++i) {                                                                  \
      const v4u v = VB[i];                                                                                           \
      o[2 * i] = mfma16(A, (MASK) ? v2u{v[0], v[1]} : v2u{0u, 0u});                                                  \
      o[2 * i + 1] = mfma16(A, (MASK) ? v2u{v[2], v[3]} : v2u{0u, 0u});                                              \
    }                                                                                                                \
    _Pragma("unroll") for (int r = 0; r < 4; ++r)                                                                    \
      __builtin_amdgcn_raw_buffer_store_b128(v4u{cvt_pk(o[0][r], o[1][r]), cvt_pk(o[2][r], o[3][r]), cvt_pk(o[4][r], o[5][r]), cvt_pk(o[6][r], o[7][r])}, \
                                             ro, ob + r * 768 + (OFFS), 0, AUX_OUT);                                  \
  }
  PV_GROUP(pe, vb[0], true, 0)
  PV_GROUP(po, vb[1], true, 512)
  const bool mine = (n < 8) == (lane < 32);
  PV_GROUP(pa, vb[2], mine, 256)
}

extern "C" void kernel_launch(void* const* d_in, const int* in_sizes, int n_in, void* d_out, int out_size, void* d_ws, size_t ws_size,
                              hipStream_t stream) {
  (void)in_sizes; (void)n_in; (void)out_size; (void)ws_size;
  (void)hipFuncSetAttribute((const void*)linformer_fused, hipFuncAttributeMaxDynamicSharedMemorySize, LDS_BYTES);
  linformer_fused<<<NWG, NTHR, LDS_BYTES, stream>>>((const uint16_t*)d_in[0], (const uint16_t*)d_in[1], (const uint16_t*)d_in[2],
                                                    (const uint16_t*)d_in[3], (const uint16_t*)d_in[4], (uint16_t*)d_out, (unsigned char*)d_ws);
}
```

```cpp
#include <hip/hip_runtime.h>
#include <hip/hip_bf16.h>
#include <stdint.h>

#define GAS __attribute__((address_space(1)))
#define LAS __attribute__((address_space(3)))
typedef unsigned int u32;
typedef unsigned long long u64;
typedef u32 v4u __attribute__((ext_vector_type(4)));
typedef u32 v2u __attribute__((ext_vector_type(2)));
typedef float f32x4 __attribute__((ext_vector_type(4)));
typedef float f32x2 __attribute__((ext_vector_type(2)));
typedef short s16x8 __attribute__((ext_vector_type(8)));
typedef short s16x4 __attribute__((ext_vector_type(4)));
typedef __bf16 bf16x2 __attribute__((ext_vector_type(2)));
typedef GAS u64 gu64;
typedef __amdgpu_buffer_rsrc_t rsrc_t;

constexpr int H = 32, S = 2048, D = 192, R = 8;
constexpr int ROWB = D * 2;
constexpr u32 TENS_BYTES = (u32)H * S * ROWB;
constexpr int NWG = 256, NTHR = 512;
constexpr int WIN = 4;
constexpr int QAHEAD = 3;
constexpr int AUX_OUT = 17;
constexpr u32 RSRC3 = 0x00020000;

constexpr size_t WS_EPOCH = 0;
constexpr size_t WS_PART = 4096;
constexpr size_t PART_HEAD = 12 * 2048;

constexpr int Q_OFF = 0;
constexpr int RED_OFF = 98304;
constexpr int KP_OFF = RED_OFF + 32768;
constexpr int KP_STRIDE = 400;
constexpr int VT_OFF = KP_OFF + 8 * KP_STRIDE;
constexpr int CTL_OFF = VT_OFF + 192 * 16;
constexpr int LDS_BYTES = CTL_OFF + 64;
enum { C_X = 0, C_K = 1, C_V = 2, C_T = 3 };

__device__ __forceinline__ u32 as_u(float x) { return __builtin_bit_cast(u32, x); }
__device__ __forceinline__ float as_f(u32 x) { return __builtin_bit_cast(float, x); }
__device__ __forceinline__ f32x4 mfma32(v4u a, v4u b, f32x4 c) {
  return __builtin_amdgcn_mfma_f32_16x16x32_bf16(__builtin_bit_cast(s16x8, a), __builtin_bit_cast(s16x8, b), c, 0, 0, 0);
}
__device__ __forceinline__ f32x4 mfma16(v2u a, v2u b) {
  return __builtin_amdgcn_mfma_f32_16x16x16bf16_1k(__builtin_bit_cast(s16x4, a), __builtin_bit_cast(s16x4, b), f32x4{0.f, 0.f, 0.f, 0.f}, 0, 0, 0);
}
__device__ __forceinline__ u32 cvt_pk(float lo, float hi) {
  const f32x2 v = {lo, hi};
  return __builtin_bit_cast(u32, __builtin_convertvector(v, bf16x2));
}
__device__ __forceinline__ float bf_lo(u32 x) { return as_f(x << 16); }
__device__ __forceinline__ float bf_hi(u32 x) { return as_f(x & 0xffff0000u); }
__device__ __forceinline__ float ror8(float x) {
  return as_f((u32)__builtin_amdgcn_update_dpp(0, (int)as_u(x), 0x128, 0xf, 0xf, true));
}
__device__ __forceinline__ float ror8_hi(float old, float x) {
  return as_f((u32)__builtin_amdgcn_update_dpp((int)as_u(old), (int)as_u(x), 0x128, 0xf, 0xc, false));
}

template <int IMM> __device__ __forceinline__ void ld_nt(v4u& d, u32 voff, rsrc_t rs, u32 soff) {
  asm volatile("buffer_load_dwordx4 %0, %1, %2, %3 offen offset:%4 nt" : "=v"(d) : "v"(voff), "s"(rs), "s"(soff), "i"(IMM));
}
__device__ __forceinline__ void ld_plain(v4u& d, u32 voff, rsrc_t rs, u32 soff) {
  asm volatile("buffer_load_dwordx4 %0, %1, %2, %3 offen" : "=v"(d) : "v"(voff), "s"(rs), "s"(soff));
}
__device__ __forceinline__ void dma_nt(u32 lds_dst, u32 voff, rsrc_t rs, u32 soff) {
  asm volatile("s_mov_b32 m0, %0\n\ts_nop 0\n\tbuffer_load_dwordx4 %1, %2, %3 offen nt lds" ::"s"(lds_dst), "v"(voff), "s"(rs), "s"(soff) : "memory");
}
template <int N> __device__ __forceinline__ void wait_vm() { asm volatile("s_waitcnt vmcnt(%0)" ::"i"(N) : "memory"); }
template <int N> __device__ __forceinline__ void wait_vm_plus(u32 sel) {
  asm volatile("s_waitcnt vmcnt(%1)\n\ts_cmp_lg_u32 %0, 0\n\ts_cbranch_scc1 1f\n\ts_waitcnt vmcnt(%2)\n1:" ::"s"(sel), "i"(N + 1), "i"(N) : "memory", "scc");
}
__device__ __forceinline__ void wait_lds() { asm volatile("s_waitcnt lgkmcnt(0)" ::: "memory"); }
#define CFENCE() asm volatile("" ::: "memory")

__device__ __forceinline__ u32 lds_ld(const LAS u32* p) {
  return (u32)__builtin_amdgcn_readfirstlane((int)__hip_atomic_load(p, __ATOMIC_RELAXED, __HIP_MEMORY_SCOPE_WORKGROUP));
}
__device__ __forceinline__ void lds_inc(LAS u32* p) {
  asm volatile("ds_add_u32 %0, %1" ::"v"((u32)(size_t)p), "v"(1u) : "memory");
}
__device__ __forceinline__ void lds_wait_ge(const LAS u32* p, u32 n) {
  _Pragma("nounroll") for (u32 spins = 0; spins < (1u << 24); ++spins)
    if (lds_ld(p) >= n) break;
  CFENCE();
}

__device__ __forceinline__ u32 q_piece_off(int i, int g, int n) {
  const int G = 4 * i + g, rho = (G * 43) >> 7;
  return (u32)(256 * G + 16 * (n ^ rho));
}

__device__ __forceinline__ void proj_pack(const f32x4 (&acc)[8], f32x4 (&z)[2]) {
#pragma unroll
  for (int e = 0; e < 2; ++e)
#pragma unroll
    for (int r = 0; r < 4; ++r) {
      auto s0 = __builtin_amdgcn_permlane32_swap(as_u(acc[4 * e + 0][r]), as_u(acc[4 * e + 1][r]), false, false);
      auto s1 = __builtin_amdgcn_permlane32_swap(as_u(acc[4 * e + 2][r]), as_u(acc[4 * e + 3][r]), false, false);
      const float x = as_f(s0[0]) + ror8(as_f(s0[1]));
      const float y = as_f(s1[0]) + ror8(as_f(s1[1]));
      z[e][r] = ror8_hi(x, y);
    }
}

__global__ void __launch_bounds__(NTHR, 2)
linformer_fused(const uint16_t* __restrict__ Qp, const uint16_t* __restrict__ Kp, const uint16_t* __restrict__ Vp,
                const uint16_t* __restrict__ Ep, const uint16_t* __restrict__ Fp, uint16_t* __restrict__ Op, unsigned char* __restrict__ ws) {
  extern __shared__ __attribute__((aligned(16))) unsigned char smem[];
  LAS unsigned char* L = (LAS unsigned char*)smem;
  const int b = blockIdx.x;
  const int h = (b & 7) * 4 + (b >> 6), j = (b >> 3) & 7;
  const int t = j >> 2, jj = j & 3;
  const int tid = threadIdx.x, lane = tid & 63, w = __builtin_amdgcn_readfirstlane(tid >> 6);
  const int g = lane >> 4, n = lane & 15;
  const u32 ldsb = (u32)(size_t)smem;
  LAS u32* ctl = (LAS u32*)(L + CTL_OFF);
  const u32 qtile = (u32)((h * S + 256 * j + 32 * w) * ROWB);
  const rsrc_t rx = __builtin_amdgcn_make_buffer_rsrc((void*)(t ? Vp : Kp), 0, TENS_BYTES, RSRC3);
  const rsrc_t rp = __builtin_amdgcn_make_buffer_rsrc((void*)(t ? Fp : Ep), 0, R * S * 2, RSRC3);
  const rsrc_t rq = __builtin_amdgcn_make_buffer_rsrc((void*)Qp, 0, TENS_BYTES, RSRC3);
  const rsrc_t ro = __builtin_amdgcn_make_buffer_rsrc((void*)Op, 0, TENS_BYTES, RSRC3);
  const rsrc_t rw = __builtin_amdgcn_make_buffer_rsrc((void*)ws, 0, 4096, RSRC3);

  const int u0 = 24 * jj + 3 * w;
  const u32 voffE = (u32)((lane & 7) * (S * 2) + (16 * g + 8 * ((lane >> 3) & 1)) * 2);
  const u32 voffX = (u32)((16 * g + 8 * ((lane >> 3) & 1)) * ROWB + 16 * (lane & 7));
  v4u ef[3], x[3][8];
  u32 so[3], ep;
#pragma unroll
  for (int i = 0; i < 3; ++i) so[i] = (u32)((h * S + ((u0 + i) & 31) * 64) * ROWB + 128 * ((u0 + i) >> 5));
#define LDX(i, k) { wait_vm<WIN - 1>(); ld_nt<(k) * ROWB>(x[i][k], voffX, rx, so[i]); }
#define LDXE(i, k) { wait_vm<WIN>(); ld_nt<(k) * ROWB>(x[i][k], voffX, rx, so[i]); }
#define LDE(i) ld_plain(ef[i], voffE, rp, (u32)(((u0 + i) & 31) * 128));
#define LDQ(i) { wait_vm<WIN - 1>(); dma_nt(ldsb + Q_OFF + w * 12288 + (i) * 1024, q_piece_off((i), g, n), rq, qtile); }
#define WAITX(i, N)                                                                                                  \
  asm volatile("s_waitcnt vmcnt(%9)"                                                                                 \
               : "+v"(x[i][0]), "+v"(x[i][1]), "+v"(x[i][2]), "+v"(x[i][3]), "+v"(x[i][4]), "+v"(x[i][5]), "+v"(x[i][6]), "+v"(x[i][7]), "+v"(ef[i]) \
               : "i"(N))
  ld_nt<0>(x[0][0], voffX, rx, so[0]);
  ld_nt<ROWB>(x[0][1], voffX, rx, so[0]);
  LDE(0)
  if (w == 0 && lane < 4) __hip_atomic_store(ctl + lane, 0u, __ATOMIC_RELAXED, __HIP_MEMORY_SCOPE_WORKGROUP);
  const int slo = (24 * jj) >> 5;
  const int sA = u0 >> 5, s1 = (u0 + 1) >> 5, sB = (u0 + 2) >> 5;
  LAS f32x4* red = (LAS f32x4*)(L + RED_OFF);
  {
    const f32x4 z0 = {0.f, 0.f, 0.f, 0.f};
    if (sA != slo) { red[(0 * 8 + w) * 128 + lane] = z0; red[(0 * 8 + w) * 128 + 64 + lane] = z0; }
    if (sB == slo) { red[(1 * 8 + w) * 128 + lane] = z0; red[(1 * 8 + w) * 128 + 64 + lane] = z0; }
  }
  wait_lds();
  __builtin_amdgcn_s_barrier();
  CFENCE();
  LDXE(0, 2) LDXE(0, 3) LDXE(0, 4) LDXE(0, 5) LDX(0, 6) LDX(0, 7)
  LDE(1) LDXE(1, 0) LDXE(1, 1)

  f32x4 acc[8];
#define UNIT(i, ZERO)                                                                                                \
  _Pragma("unroll") for (int dd = 0; dd < 8; ++dd) {                                                                 \
    v4u f;                                                                                                           \
    _Pragma("unroll") for (int k = 0; k < 4; ++k)                                                                    \
        f[k] = __builtin_amdgcn_perm(x[i][2 * k + 1][dd >> 1], x[i][2 * k][dd >> 1], (dd & 1) ? 0x07060302u : 0x05040100u); \
    acc[dd] = mfma32(ef[i], f, ZERO ? f32x4{0.f, 0.f, 0.f, 0.f} : acc[dd]);                                          \
  }
#define FLUSH(strip)                                                                                                 \
  {                                                                                                                  \
    f32x4 z[2];                                                                                                      \
    proj_pack(acc, z);                                                                                               \
    const int sl = ((strip) != slo) ? 1 : 0;                                                                         \
    red[(sl * 8 + w) * 128 + lane] = z[0];                                                                           \
    red[(sl * 8 + w) * 128 + 64 + lane] = z[1];                                                                      \
  }
#define CLEAR() _Pragma("unroll") for (int dd = 0; dd < 8; ++dd) acc[dd] = f32x4{0.f, 0.f, 0.f, 0.f};
  WAITX(0, 3);
  LDXE(1, 2)
  UNIT(0, true)
  if (s1 != sA) { FLUSH(sA) CLEAR() }
  LDXE(1, 3) LDX(1, 4) LDX(1, 5) LDX(1, 6) LDX(1, 7)
  LDE(2) LDXE(2, 0) LDXE(2, 1)
  WAITX(1, 3);
  LDXE(2, 2)
  UNIT(1, false)
  if (sB != s1) { FLUSH(s1) CLEAR() }
  LDXE(2, 3) LDX(2, 4) LDX(2, 5) LDX(2, 6) LDX(2, 7)
  wait_vm<WIN - 1>();
  asm volatile("buffer_load_dword %0, off, %1, %2 sc1" : "=v"(ep) : "s"(rw), "s"((u32)(WS_EPOCH + h * 128)));
  LDQ(0) LDQ(1)
  WAITX(2, 3);
  LDQ(2)
  UNIT(2, false)
  FLUSH(sB)
  wait_lds();
  if (lane == 0) lds_inc(ctl + C_X);
  asm volatile("s_waitcnt vmcnt(%1)" : "+v"(ep) : "i"(QAHEAD));
  const u32 tag = (u32)__builtin_amdgcn_readfirstlane(ep) + 1u;
  gu64* gran = (gu64*)(ws + WS_PART + h * PART_HEAD);

  auto publish = [&]() {
    CFENCE();
    const int half = lane >> 5, which = (lane >> 4) & 1, it = 16 * w + (lane & 15);
    f32x4 v = red[(which * 8 + 4 * half + 0) * 128 + it];
#pragma unroll
    for (int k = 1; k < 4; ++k) v += red[(which * 8 + 4 * half + k) * 128 + it];
    f32x4 tot;
#pragma unroll
    for (int r = 0; r < 4; ++r) {
      auto sw = __builtin_amdgcn_permlane32_swap(as_u(v[r]), as_u(v[r]), false, false);
      tot[r] = as_f(sw[0]) + as_f(sw[1]);
    }
    const int slot = 6 * t + 2 * jj - (jj > 0 ? 1 : 0) + which;
    const u32 pk = half ? cvt_pk(tot[2], tot[3]) : cvt_pk(tot[0], tot[1]);
    if (which == 0 || jj == 1 || jj == 2)
      __hip_atomic_store(gran + slot * 256 + half * 128 + it, ((u64)tag << 32) | pk, __ATOMIC_RELAXED, __HIP_MEMORY_SCOPE_AGENT);
  };
  int published = 0;
  _Pragma("nounroll") for (int i = QAHEAD; i < 12; ++i) {
    published = __builtin_amdgcn_readfirstlane(published);
    u32 cx;
    asm volatile("ds_read_b32 %0, %1" : "=v"(cx) : "v"(ldsb + CTL_OFF + 4 * C_X) : "memory");
    wait_vm_plus<WIN - 1>((u32)published);
    dma_nt(ldsb + Q_OFF + w * 12288 + i * 1024, q_piece_off(i, g, n), rq, qtile);
    asm volatile("s_waitcnt lgkmcnt(0)" : "+v"(cx)::"memory");
    if (!published && __builtin_amdgcn_readfirstlane((int)cx) >= 8) { publish(); published = 1; }
  }
  published = __builtin_amdgcn_readfirstlane(published);
  if (!published) {
    lds_wait_ge(ctl + C_X, 8u);
    publish();
  }
  wait_vm<0>();

  u32 tk = 0;
  if (lane == 0) tk = __hip_atomic_fetch_add(ctl + C_T, 1u, __ATOMIC_RELAXED, __HIP_MEMORY_SCOPE_WORKGROUP);
  const int ts = __builtin_amdgcn_readfirstlane((int)tk);
  u64 gr[2][2][2];
  const gu64* gp = gran + (2 * ts) * 256 + lane;
  if (ts < 6) {
#pragma unroll
    for (int p = 0; p < 2; ++p)
#pragma unroll
      for (int hf = 0; hf < 2; ++hf)
#pragma unroll
        for (int e = 0; e < 2; ++e) gr[p][hf][e] = __hip_atomic_load(gp + p * 256 + hf * 128 + e * 64, __ATOMIC_RELAXED, __HIP_MEMORY_SCOPE_AGENT);
    CFENCE();
  }
  v4u qf[12];
  {
    const LAS unsigned char* qrow = L + Q_OFF + w * 12288 + n * 768;
#pragma unroll
    for (int s = 0; s < 6; ++s) {
      qf[s] = *(const LAS v4u*)(qrow + 16 * ((4 * s + g) ^ n));
      qf[6 + s] = *(const LAS v4u*)(qrow + 16 * ((24 + 4 * s + g) ^ n));
    }
  }
  if (ts < 6) {
    const int tt = ts >= 3 ? 1 : 0, s = ts - 3 * tt;
    const int ch = lane & 7, be = (lane >> 3) & 1, ga = (lane >> 4) & 1, et = lane >> 5;
    u32 dst[2];
#pragma unroll
    for (int e = 0; e < 2; ++e) {
      const int d = 64 * s + 8 * ch + 4 * e + 2 * be + et;
      dst[e] = ldsb + (tt == 0 ? KP_OFF + (4 * ga) * KP_STRIDE + 2 * d : VT_OFF + ((d & 7) >> 1) * 768 + (d >> 3) * 32 + ga * 16 + (d & 1) * 8);
    }
    asm volatile("" : "+v"(dst[0]), "+v"(dst[1]));
    _Pragma("nounroll") for (u32 spins = 0; spins < (1u << 20); ++spins) {
      bool ok[2][2], all = true;
#pragma unroll
      for (int p = 0; p < 2; ++p)
#pragma unroll
        for (int e = 0; e < 2; ++e) { ok[p][e] = ((u32)(gr[p][0][e] >> 32) == tag) & ((u32)(gr[p][1][e] >> 32) == tag); all &= ok[p][e]; }
      if (__all(all)) break;
      __builtin_amdgcn_s_sleep(6);
#pragma unroll
      for (int p = 0; p < 2; ++p)
#pragma unroll
        for (int e = 0; e < 2; ++e)
          if (!ok[p][e]) {
            gr[p][0][e] = __hip_atomic_load(gp + p * 256 + e * 64, __ATOMIC_RELAXED, __HIP_MEMORY_SCOPE_AGENT);
            gr[p][1][e] = __hip_atomic_load(gp + p * 256 + 128 + e * 64, __ATOMIC_RELAXED, __HIP_MEMORY_SCOPE_AGENT);
          }
    }
#pragma unroll
    for (int e = 0; e < 2; ++e) {
      const u32 a0 = (u32)gr[0][0][e], a1 = (u32)gr[0][1][e], b0 = (u32)gr[1][0][e], b1 = (u32)gr[1][1][e];
      const u32 v01 = cvt_pk(bf_lo(a0) + bf_lo(b0), bf_hi(a0) + bf_hi(b0));
      const u32 v23 = cvt_pk(bf_lo(a1) + bf_lo(b1), bf_hi(a1) + bf_hi(b1));
      if (tt == 0) {
        LAS uint16_t* kp = (LAS uint16_t*)(size_t)dst[e];
        kp[0] = (uint16_t)v01;
        kp[KP_STRIDE / 2] = (uint16_t)(v01 >> 16);
        kp[KP_STRIDE] = (uint16_t)v23;
        kp[3 * KP_STRIDE / 2] = (uint16_t)(v23 >> 16);
      } else {
        *(LAS v2u*)(size_t)dst[e] = v2u{v01, v23};
      }
    }
    wait_lds();
    if (lane == 0) lds_inc(ctl + (ts < 3 ? C_K : C_V));
  }

  const LAS unsigned char* kfrag = L + KP_OFF + (n & 7) * KP_STRIDE + 16 * g;
  const LAS unsigned char* vfragA = L + VT_OFF + n * 32 + (g & 1) * 16;
  const LAS unsigned char* vfragB = L + VT_OFF + (n < 8 ? 16 + n : n - 8) * 32 + (g & 1) * 16;
  const LAS unsigned char* vfragC = L + VT_OFF + (8 + n) * 32 + (g & 1) * 16;
  const u32 ob = qtile + (4 * g) * 768 + 16 * n;
#pragma unroll
  for (int s = 0; s < 12; ++s) asm volatile("" : "+v"(qf[s]));
  lds_wait_ge(ctl + C_K, 3u);
  f32x4 d0 = {0.f, 0.f, 0.f, 0.f}, d1 = {0.f, 0.f, 0.f, 0.f};
  {
    v4u F[6];
#pragma unroll
    for (int s = 0; s < 6; ++s) F[s] = *(const LAS v4u*)(kfrag + 64 * s);
#pragma unroll
    for (int s = 0; s < 6; ++s) {
      d0 = mfma32(F[s], qf[s], d0);
      d1 = mfma32(F[s], qf[6 + s], d1);
    }
  }
  lds_wait_ge(ctl + C_V, 3u);
  v4u vb[3][4];
#pragma unroll
  for (int i = 0; i < 4; ++i) { vb[0][i] = *(const LAS v4u*)(vfragA + i * 768); vb[1][i] = *(const LAS v4u*)(vfragC + i * 768); vb[2][i] = *(const LAS v4u*)(vfragB + i * 768); }
  if (j == 0 && tid == 0) __hip_atomic_store((GAS u32*)(ws + WS_EPOCH + h * 128), tag, __ATOMIC_RELAXED, __HIP_MEMORY_SCOPE_AGENT);
  const float sc = 0.07216878364870322f * 1.4426950408889634f;
  float sv[4];
#pragma unroll
  for (int r = 0; r < 4; ++r) sv[r] = (lane < 32 ? d0[r] : d1[r]);
  float m = fmaxf(fmaxf(sv[0], sv[1]), fmaxf(sv[2], sv[3]));
  {
    auto x2 = __builtin_amdgcn_permlane16_swap(as_u(m), as_u(m), false, false);
    m = fmaxf(as_f(x2[0]), as_f(x2[1]));
  }
  float ps[4];
#pragma unroll
  for (int r = 0; r < 4; ++r) ps[r] = __builtin_amdgcn_exp2f((sv[r] - m) * sc);
  float sum = (ps[0] + ps[1]) + (ps[2] + ps[3]);
  {
    auto x2 = __builtin_amdgcn_permlane16_swap(as_u(sum), as_u(sum), false, false);
    sum = as_f(x2[0]) + as_f(x2[1]);
  }
  const float inv = __builtin_amdgcn_rcpf(sum);
  const v2u pa = v2u{cvt_pk(ps[0] * inv, ps[1] * inv), cvt_pk(ps[2] * inv, ps[3] * inv)};
  const v2u pe = lane < 32 ? pa : v2u{0u, 0u}, po = lane < 32 ? v2u{0u, 0u} : pa;
#define PV_GROUP(A, VB, MASK, OFFS)                                                                                  \
  {                                                                                                                  \
    f32x4 o[8];                                                                                                      \
    _Pragma("unroll") for (int i = 0; i < 4; ++i) {                                                                  \
      const v4u v = VB[i];                                                                                           \
      o[2 * i] = mfma16(A, (MASK) ? v2u{v[0], v[1]} : v2u{0u, 0u});                                                  \
      o[2 * i + 1] = mfma16(A, (MASK) ? v2u{v[2], v[3]} : v2u{0u, 0u});                                              \
    }                                                                                                                \
    _Pragma("unroll") for (int r = 0; r < 4; ++r)                                                                    \
      __builtin_amdgcn_raw_buffer_store_b128(v4u{cvt_pk(o[0][r], o[1][r]), cvt_pk(o[2][r], o[3][r]), cvt_pk(o[4][r], o[5][r]), cvt_pk(o[6][r], o[7][r])}, \
                                             ro, ob + r * 768 + (OFFS), 0, AUX_OUT);                                  \
  }
  PV_GROUP(pe, vb[0], true, 0)
  PV_GROUP(po, vb[1], true, 512)
  const bool mine = (n < 8) == (lane < 32);
  PV_GROUP(pa, vb[2], mine, 256)
}

extern "C" void kernel_launch(void* const* d_in, const int* in_sizes, int n_in, void* d_out, int out_size, void* d_ws, size_t ws_size,
                              hipStream_t stream) {
  (void)in_sizes; (void)n_in; (void)out_size; (void)ws_size;
  (void)hipFuncSetAttribute((const void*)linformer_fused, hipFuncAttributeMaxDynamicSharedMemorySize, LDS_BYTES);
  linformer_fused<<<NWG, NTHR, LDS_BYTES, stream>>>((const uint16_t*)d_in[0], (const uint16_t*)d_in[1], (const uint16_t*)d_in[2],
                                                    (const uint16_t*)d_in[3], (const uint16_t*)d_in[4], (uint16_t*)d_out, (unsigned char*)d_ws);
}
```

```cpp
#include <hip/hip_runtime.h>
#include <hip/hip_bf16.h>
#include <stdint.h>

#define GAS __attribute__((address_space(1)))
#define LAS __attribute__((address_space(3)))
typedef unsigned int u32;
typedef unsigned long long u64;
typedef u32 v4u __attribute__((ext_vector_type(4)));
typedef u32 v2u __attribute__((ext_vector_type(2)));
typedef float f32x4 __attribute__((ext_vector_type(4)));
typedef float f32x2 __attribute__((ext_vector_type(2)));
typedef short s16x8 __attribute__((ext_vector_type(8)));
typedef short s16x4 __attribute__((ext_vector_type(4)));
typedef __bf16 bf16x2 __attribute__((ext_vector_type(2)));
typedef GAS u64 gu64;
typedef __amdgpu_buffer_rsrc_t rsrc_t;

constexpr int H = 32, S = 2048, D = 192, R = 8;
constexpr int ROWB = D * 2;
constexpr u32 TENS_BYTES = (u32)H * S * ROWB;
constexpr int NWG = 256, NTHR = 512;
constexpr int WIN = 4;
constexpr int QAHEAD = 3;
constexpr int AUX_OUT = 17;
constexpr u32 RSRC3 = 0x00020000;

constexpr size_t WS_EPOCH = 0;
constexpr size_t WS_PART = 4096;
constexpr size_t PART_HEAD = 12 * 2048;

constexpr int Q_OFF = 0;
constexpr int RED_OFF = 98304;
constexpr int KP_OFF = RED_OFF + 32768;
constexpr int VT_OFF = KP_OFF + 24 * 144;
constexpr int CTL_OFF = VT_OFF + 192 * 16;
constexpr int LDS_BYTES = CTL_OFF + 64;
enum { C_X = 0, C_K = 1, C_V = 2, C_T = 3 };

__device__ __forceinline__ u32 as_u(float x) { return __builtin_bit_cast(u32, x); }
__device__ __forceinline__ float as_f(u32 x) { return __builtin_bit_cast(float, x); }
__device__ __forceinline__ f32x4 mfma32(v4u a, v4u b, f32x4 c) {
  return __builtin_amdgcn_mfma_f32_16x16x32_bf16(__builtin_bit_cast(s16x8, a), __builtin_bit_cast(s16x8, b), c, 0, 0, 0);
}
__device__ __forceinline__ f32x4 mfma16(v2u a, v2u b) {
  return __builtin_amdgcn_mfma_f32_16x16x16bf16_1k(__builtin_bit_cast(s16x4, a), __builtin_bit_cast(s16x4, b), f32x4{0.f, 0.f, 0.f, 0.f}, 0, 0, 0);
}
__device__ __forceinline__ u32 cvt_pk(float lo, float hi) {
  const f32x2 v = {lo, hi};
  return __builtin_bit_cast(u32, __builtin_convertvector(v, bf16x2));
}
__device__ __forceinline__ float bf_lo(u32 x) { return as_f(x << 16); }
__device__ __forceinline__ float bf_hi(u32 x) { return as_f(x & 0xffff0000u); }
__device__ __forceinline__ float ror8(float x) {
  return as_f((u32)__builtin_amdgcn_update_dpp(0, (int)as_u(x), 0x128, 0xf, 0xf, true));
}
__device__ __forceinline__ float ror8_hi(float old, float x) {
  return as_f((u32)__builtin_amdgcn_update_dpp((int)as_u(old), (int)as_u(x), 0x128, 0xf, 0xc, false));
}

template <int IMM> __device__ __forceinline__ void ld_nt(v4u& d, u32 voff, rsrc_t rs, u32 soff) {
  asm volatile("buffer_load_dwordx4 %0, %1, %2, %3 offen offset:%4 nt" : "=v"(d) : "v"(voff), "s"(rs), "s"(soff), "i"(IMM));
}
__device__ __forceinline__ void ld_plain(v4u& d, u32 voff, rsrc_t rs, u32 soff) {
  asm volatile("buffer_load_dwordx4 %0, %1, %2, %3 offen" : "=v"(d) : "v"(voff), "s"(rs), "s"(soff));
}
__device__ __forceinline__ void dma_nt(u32 lds_dst, u32 voff, rsrc_t rs, u32 soff) {
  asm volatile("s_mov_b32 m0, %0\n\ts_nop 0\n\tbuffer_load_dwordx4 %1, %2, %3 offen nt lds" ::"s"(lds_dst), "v"(voff), "s"(rs), "s"(soff) : "memory");
}
template <int N> __device__ __forceinline__ void wait_vm() { asm volatile("s_waitcnt vmcnt(%0)" ::"i"(N) : "memory"); }
template <int N> __device__ __forceinline__ void wait_vm_plus(u32 sel) {
  asm volatile("s_waitcnt vmcnt(%1)\n\ts_cmp_lg_u32 %0, 0\n\ts_cbranch_scc1 1f\n\ts_waitcnt vmcnt(%2)\n1:" ::"s"(sel), "i"(N + 1), "i"(N) : "memory", "scc");
}
__device__ __forceinline__ void wait_lds() { asm volatile("s_waitcnt lgkmcnt(0)" ::: "memory"); }
#define CFENCE() asm volatile("" ::: "memory")

__device__ __forceinline__ u32 lds_ld(const LAS u32* p) {
  return (u32)__builtin_amdgcn_readfirstlane((int)__hip_atomic_load(p, __ATOMIC_RELAXED, __HIP_MEMORY_SCOPE_WORKGROUP));
}
__device__ __forceinline__ void lds_inc(LAS u32* p) {
  asm volatile("ds_add_u32 %0, %1" ::"v"((u32)(size_t)p), "v"(1u) : "memory");
}
__device__ __forceinline__ void lds_wait_ge(const LAS u32* p, u32 n) {
  _Pragma("nounroll") for (u32 spins = 0; spins < (1u << 24); ++spins)
    if (lds_ld(p) >= n) break;
  CFENCE();
}

__device__ __forceinline__ u32 q_piece_off(int i, int g, int n) {
  const int G = 4 * i + g, rho = (G * 43) >> 7;
  return (u32)(256 * G + 16 * (n ^ rho));
}

__device__ __forceinline__ void proj_pack(const f32x4 (&acc)[8], f32x4 (&z)[2]) {
#pragma unroll
  for (int e = 0; e < 2; ++e)
#pragma unroll
    for (int r = 0; r < 4; ++r) {
      auto s0 = __builtin_amdgcn_permlane32_swap(as_u(acc[4 * e + 0][r]), as_u(acc[4 * e + 1][r]), false, false);
      auto s1 = __builtin_amdgcn_permlane32_swap(as_u(acc[4 * e + 2][r]), as_u(acc[4 * e + 3][r]), false, false);
      const float x = as_f(s0[0]) + ror8(as_f(s0[1]));
      const float y = as_f(s1[0]) + ror8(as_f(s1[1]));
      z[e][r] = ror8_hi(x, y);
    }
}

__global__ void __launch_bounds__(NTHR, 2)
linformer_fused(const uint16_t* __restrict__ Qp, const uint16_t* __restrict__ Kp, const uint16_t* __restrict__ Vp,
                const uint16_t* __restrict__ Ep, const uint16_t* __restrict__ Fp, uint16_t* __restrict__ Op, unsigned char* __restrict__ ws) {
  extern __shared__ __attribute__((aligned(16))) unsigned char smem[];
  LAS unsigned char* L = (LAS unsigned char*)smem;
  const int b = blockIdx.x;
  const int h = (b & 7) * 4 + (b >> 6), j = (b >> 3) & 7;
  const int t = j >> 2, jj = j & 3;
  const int tid = threadIdx.x, lane = tid & 63, w = __builtin_amdgcn_readfirstlane(tid >> 6);
  const int g = lane >> 4, n = lane & 15;
  const u32 ldsb = (u32)(size_t)smem;
  LAS u32* ctl = (LAS u32*)(L + CTL_OFF);
  const u32 qtile = (u32)((h * S + 256 * j + 32 * w) * ROWB);
  const rsrc_t rx = __builtin_amdgcn_make_buffer_rsrc((void*)(t ? Vp : Kp), 0, TENS_BYTES, RSRC3);
  const rsrc_t rp = __builtin_amdgcn_make_buffer_rsrc((void*)(t ? Fp : Ep), 0, R * S * 2, RSRC3);
  const rsrc_t rq = __builtin_amdgcn_make_buffer_rsrc((void*)Qp, 0, TENS_BYTES, RSRC3);
  const rsrc_t ro = __builtin_amdgcn_make_buffer_rsrc((void*)Op, 0, TENS_BYTES, RSRC3);
  const rsrc_t rw = __builtin_amdgcn_make_buffer_rsrc((void*)ws, 0, 4096, RSRC3);

  const int u0 = 24 * jj + 3 * w;
  const u32 voffE = (u32)((lane & 7) * (S * 2) + (16 * g + 8 * ((lane >> 3) & 1)) * 2);
  const u32 voffX = (u32)((16 * g + 8 * ((lane >> 3) & 1)) * ROWB + 16 * (lane & 7));
  v4u ef[3], x[3][8];
  u32 so[3], ep;
#pragma unroll
  for (int i = 0; i < 3; ++i) so[i] = (u32)((h * S + ((u0 + i) & 31) * 64) * ROWB + 128 * ((u0 + i) >> 5));
#define LDX(i, k) { wait_vm<WIN - 1>(); ld_nt<(k) * ROWB>(x[i][k], voffX, rx, so[i]); }
#define LDXE(i, k) { wait_vm<WIN>(); ld_nt<(k) * ROWB>(x[i][k], voffX, rx, so[i]); }
#define LDE(i) ld_plain(ef[i], voffE, rp, (u32)(((u0 + i) & 31) * 128));
#define LDQ(i) { wait_vm<WIN - 1>(); dma_nt(ldsb + Q_OFF + w * 12288 + (i) * 1024, q_piece_off((i), g, n), rq, qtile); }
#define WAITX(i, N)                                                                                                  \
  asm volatile("s_waitcnt vmcnt(%9)"                                                                                 \
               : "+v"(x[i][0]), "+v"(x[i][1]), "+v"(x[i][2]), "+v"(x[i][3]), "+v"(x[i][4]), "+v"(x[i][5]), "+v"(x[i][6]), "+v"(x[i][7]), "+v"(ef[i]) \
               : "i"(N))
  ld_nt<0>(x[0][0], voffX, rx, so[0]);
  ld_nt<ROWB>(x[0][1], voffX, rx, so[0]);
  LDE(0)
  if (w == 0 && lane < 4) __hip_atomic_store(ctl + lane, 0u, __ATOMIC_RELAXED, __HIP_MEMORY_SCOPE_WORKGROUP);
  const int slo = (24 * jj) >> 5;
  const int sA = u0 >> 5, s1 = (u0 + 1) >> 5, sB = (u0 + 2) >> 5;
  LAS f32x4* red = (LAS f32x4*)(L + RED_OFF);
  {
    const f32x4 z0 = {0.f, 0.f, 0.f, 0.f};
    if (sA != slo) { red[(0 * 8 + w) * 128 + lane] = z0; red[(0 * 8 + w) * 128 + 64 + lane] = z0; }
    if (sB == slo) { red[(1 * 8 + w) * 128 + lane] = z0; red[(1 * 8 + w) * 128 + 64 + lane] = z0; }
  }
  wait_lds();
  __builtin_amdgcn_s_barrier();
  CFENCE();
  LDXE(0, 2) LDXE(0, 3) LDXE(0, 4) LDXE(0, 5) LDX(0, 6) LDX(0, 7)
  LDE(1) LDXE(1, 0) LDXE(1, 1)

  f32x4 acc[8];
#define UNIT(i, ZERO)                                                                                                \
  _Pragma("unroll") for (int dd = 0; dd < 8; ++dd) {                                                                 \
    v4u f;                                                                                                           \
    _Pragma("unroll") for (int k = 0; k < 4; ++k)                                                                    \
        f[k] = __builtin_amdgcn_perm(x[i][2 * k + 1][dd >> 1], x[i][2 * k][dd >> 1], (dd & 1) ? 0x07060302u : 0x05040100u); \
    acc[dd] = mfma32(ef[i], f, ZERO ? f32x4{0.f, 0.f, 0.f, 0.f} : acc[dd]);                                          \
  }
#define FLUSH(strip)                                                                                                 \
  {                                                                                                                  \
    f32x4 z[2];                                                                                                      \
    proj_pack(acc, z);                                                                                               \
    const int sl = ((strip) != slo) ? 1 : 0;                                                                         \
    red[(sl * 8 + w) * 128 + lane] = z[0];                                                                           \
    red[(sl * 8 + w) * 128 + 64 + lane] = z[1];                                                                      \
  }
#define CLEAR() _Pragma("unroll") for (int dd = 0; dd < 8; ++dd) acc[dd] = f32x4{0.f, 0.f, 0.f, 0.f};
  WAITX(0, 3);
  LDXE(1, 2)
  UNIT(0, true)
  if (s1 != sA) { FLUSH(sA) CLEAR() }
  LDXE(1, 3) LDX(1, 4) LDX(1, 5) LDX(1, 6) LDX(1, 7)
  LDE(2) LDXE(2, 0) LDXE(2, 1)
  WAITX(1, 3);
  LDXE(2, 2)
  UNIT(1, false)
  if (sB != s1) { FLUSH(s1) CLEAR() }
  LDXE(2, 3) LDX(2, 4) LDX(2, 5) LDX(2, 6) LDX(2, 7)
  wait_vm<WIN - 1>();
  asm volatile("buffer_load_dword %0, off, %1, %2 sc1" : "=v"(ep) : "s"(rw), "s"((u32)(WS_EPOCH + h * 128)));
  LDQ(0) LDQ(1)
  WAITX(2, 3);
  LDQ(2)
  UNIT(2, false)
  FLUSH(sB)
  wait_lds();
  if (lane == 0) lds_inc(ctl + C_X);
  asm volatile("s_waitcnt vmcnt(%1)" : "+v"(ep) : "i"(QAHEAD));
  const u32 tag = (u32)__builtin_amdgcn_readfirstlane(ep) + 1u;
  gu64* gran = (gu64*)(ws + WS_PART + h * PART_HEAD);

  auto publish = [&]() {
    CFENCE();
    const int half = lane >> 5, which = (lane >> 4) & 1, it = 16 * w + (lane & 15);
    f32x4 v = red[(which * 8 + 4 * half + 0) * 128 + it];
#pragma unroll
    for (int k = 1; k < 4; ++k) v += red[(which * 8 + 4 * half + k) * 128 + it];
    f32x4 tot;
#pragma unroll
    for (int r = 0; r < 4; ++r) {
      auto sw = __builtin_amdgcn_permlane32_swap(as_u(v[r]), as_u(v[r]), false, false);
      tot[r] = as_f(sw[0]) + as_f(sw[1]);
    }
    const int slot = 6 * t + 2 * jj - (jj > 0 ? 1 : 0) + which;
    const u32 pk = half ? cvt_pk(tot[2], tot[3]) : cvt_pk(tot[0], tot[1]);
    if (which == 0 || jj == 1 || jj == 2)
      __hip_atomic_store(gran + slot * 256 + half * 128 + it, ((u64)tag << 32) | pk, __ATOMIC_RELAXED, __HIP_MEMORY_SCOPE_AGENT);
  };
  int published = 0;
  _Pragma("nounroll") for (int i = QAHEAD; i < 12; ++i) {
    published = __builtin_amdgcn_readfirstlane(published);
    u32 cx;
    asm volatile("ds_read_b32 %0, %1" : "=v"(cx) : "v"(ldsb + CTL_OFF + 4 * C_X) : "memory");
    wait_vm_plus<WIN - 1>((u32)published);
    dma_nt(ldsb + Q_OFF + w * 12288 + i * 1024, q_piece_off(i, g, n), rq, qtile);
    asm volatile("s_waitcnt lgkmcnt(0)" : "+v"(cx)::"memory");
    if (!published && __builtin_amdgcn_readfirstlane((int)cx) >= 8) { publish(); published = 1; }
  }
  published = __builtin_amdgcn_readfirstlane(published);
  if (!published) {
    lds_wait_ge(ctl + C_X, 8u);
    publish();
  }
  u32 tk = 0;
  if (lane == 0) tk = __hip_atomic_fetch_add(ctl + C_T, 1u, __ATOMIC_RELAXED, __HIP_MEMORY_SCOPE_WORKGROUP);
  const int ts = __builtin_amdgcn_readfirstlane((int)tk);
  u64 gr[2][2][2];
  const gu64* gp = gran + (2 * ts) * 256 + lane;
  asm volatile("" : "+v"(gp));
  wait_vm<0>();
  if (ts < 6) {
#pragma unroll
    for (int p = 0; p < 2; ++p)
#pragma unroll
      for (int hf = 0; hf < 2; ++hf)
#pragma unroll
        for (int e = 0; e < 2; ++e) gr[p][hf][e] = __hip_atomic_load(gp + p * 256 + hf * 128 + e * 64, __ATOMIC_RELAXED, __HIP_MEMORY_SCOPE_AGENT);
    CFENCE();
  }
  v4u qf[12];
  {
    const LAS unsigned char* qrow = L + Q_OFF + w * 12288 + n * 768;
#pragma unroll
    for (int s = 0; s < 6; ++s) {
      qf[s] = *(const LAS v4u*)(qrow + 16 * ((4 * s + g) ^ n));
      qf[6 + s] = *(const LAS v4u*)(qrow + 16 * ((24 + 4 * s + g) ^ n));
    }
  }
  if (ts < 6) {
    const int tt = ts >= 3 ? 1 : 0, s = ts - 3 * tt;
    const int ch = lane & 7, be = (lane >> 3) & 1, ga = (lane >> 4) & 1, et = lane >> 5;
    u32 dst[2];
#pragma unroll
    for (int e = 0; e < 2; ++e) {
      const int d = 64 * s + 8 * ch + 4 * e + 2 * be + et;
      dst[e] = ldsb + (tt == 0 ? KP_OFF + (d >> 3) * 144 + (d & 7) * 16 + ga * 8 : VT_OFF + ((d & 7) >> 1) * 768 + (d >> 3) * 32 + ga * 16 + (d & 1) * 8);
    }
    asm volatile("" : "+v"(dst[0]), "+v"(dst[1]));
    _Pragma("nounroll") for (u32 spins = 0; spins < (1u << 20); ++spins) {
      bool ok[2][2], all = true;
#pragma unroll
      for (int p = 0; p < 2; ++p)
#pragma unroll
        for (int e = 0; e < 2; ++e) { ok[p][e] = ((u32)(gr[p][0][e] >> 32) == tag) & ((u32)(gr[p][1][e] >> 32) == tag); all &= ok[p][e]; }
      if (__all(all)) break;
      __builtin_amdgcn_s_sleep(6);
#pragma unroll
      for (int p = 0; p < 2; ++p)
#pragma unroll
        for (int e = 0; e < 2; ++e)
          if (!ok[p][e]) {
            gr[p][0][e] = __hip_atomic_load(gp + p * 256 + e * 64, __ATOMIC_RELAXED, __HIP_MEMORY_SCOPE_AGENT);
            gr[p][1][e] = __hip_atomic_load(gp + p * 256 + 128 + e * 64, __ATOMIC_RELAXED, __HIP_MEMORY_SCOPE_AGENT);
          }
    }
#pragma unroll
    for (int e = 0; e < 2; ++e) {
      const u32 a0 = (u32)gr[0][0][e], a1 = (u32)gr[0][1][e], b0 = (u32)gr[1][0][e], b1 = (u32)gr[1][1][e];
      const u32 v01 = cvt_pk(bf_lo(a0) + bf_lo(b0), bf_hi(a0) + bf_hi(b0));
      const u32 v23 = cvt_pk(bf_lo(a1) + bf_lo(b1), bf_hi(a1) + bf_hi(b1));
      *(LAS v2u*)(size_t)dst[e] = v2u{v01, v23};
    }
    wait_lds();
    if (lane == 0) lds_inc(ctl + (ts < 3 ? C_K : C_V));
  }

  const LAS unsigned char* kfrag = L + KP_OFF + g * 144 + (n >> 2) * 16 + (n & 1) * 8;
  const LAS unsigned char* vfragA = L + VT_OFF + n * 32 + (g & 1) * 16;
  const LAS unsigned char* vfragB = L + VT_OFF + (n < 8 ? 16 + n : n - 8) * 32 + (g & 1) * 16;
  const LAS unsigned char* vfragC = L + VT_OFF + (8 + n) * 32 + (g & 1) * 16;
  const u32 ob = qtile + (4 * g) * 768 + 16 * n;
#pragma unroll
  for (int s = 0; s < 12; ++s) asm volatile("" : "+v"(qf[s]));
  lds_wait_ge(ctl + C_K, 3u);
  f32x4 d0, d1;
  {
    v4u F[6];
#pragma unroll
    for (int s = 0; s < 6; ++s) {
      const v2u lo = __builtin_bit_cast(v2u, __builtin_amdgcn_ds_read_tr16_b64_v4i16((LAS s16x4*)(kfrag + s * 576)));
      const v2u hi = __builtin_bit_cast(v2u, __builtin_amdgcn_ds_read_tr16_b64_v4i16((LAS s16x4*)(kfrag + s * 576 + 64)));
      F[s] = v4u{lo[0], lo[1], hi[0], hi[1]};
    }
    const f32x4 z4 = {0.f, 0.f, 0.f, 0.f};
    f32x4 a0 = z4, a1 = z4, b0 = z4, b1 = z4;
#pragma unroll
    for (int s = 0; s < 3; ++s) {
      a0 = mfma32(F[s], qf[s], a0);
      b0 = mfma32(F[s], qf[6 + s], b0);
      a1 = mfma32(F[3 + s], qf[3 + s], a1);
      b1 = mfma32(F[3 + s], qf[9 + s], b1);
    }
    d0 = a0 + a1;
    d1 = b0 + b1;
  }
  lds_wait_ge(ctl + C_V, 3u);
  v4u vb[3][4];
#pragma unroll
  for (int i = 0; i < 4; ++i) { vb[0][i] = *(const LAS v4u*)(vfragA + i * 768); vb[1][i] = *(const LAS v4u*)(vfragC + i * 768); vb[2][i] = *(const LAS v4u*)(vfragB + i * 768); }
  if (j == 0 && tid == 0) __hip_atomic_store((GAS u32*)(ws + WS_EPOCH + h * 128), tag, __ATOMIC_RELAXED, __HIP_MEMORY_SCOPE_AGENT);
  const float sc = 0.07216878364870322f * 1.4426950408889634f;
  float sv[4];
#pragma unroll
  for (int r = 0; r < 4; ++r) sv[r] = (lane < 32 ? d0[r] : d1[r]);
  float m = fmaxf(fmaxf(sv[0], sv[1]), fmaxf(sv[2], sv[3]));
  {
    auto x2 = __builtin_amdgcn_permlane16_swap(as_u(m), as_u(m), false, false);
    m = fmaxf(as_f(x2[0]), as_f(x2[1]));
  }
  float ps[4];
#pragma unroll
  for (int r = 0; r < 4; ++r) ps[r] = __builtin_amdgcn_exp2f((sv[r] - m) * sc);
  float sum = (ps[0] + ps[1]) + (ps[2] + ps[3]);
  {
    auto x2 = __builtin_amdgcn_permlane16_swap(as_u(sum), as_u(sum), false, false);
    sum = as_f(x2[0]) + as_f(x2[1]);
  }
  const float inv = __builtin_amdgcn_rcpf(sum);
  const v2u pa = v2u{cvt_pk(ps[0] * inv, ps[1] * inv), cvt_pk(ps[2] * inv, ps[3] * inv)};
  const v2u pe = lane < 32 ? pa : v2u{0u, 0u}, po = lane < 32 ? v2u{0u, 0u} : pa;
#define PV_GROUP(A, VB, MASK, OFFS)                                                                                  \
  {                                                                                                                  \
    f32x4 o[8];                                                                                                      \
    _Pragma("unroll") for (int i = 0; i < 4; ++i) {                                                                  \
      const v4u v = VB[i];                                                                                           \
      o[2 * i] = mfma16(A, (MASK) ? v2u{v[0], v[1]} : v2u{0u, 0u});                                                  \
      o[2 * i + 1] = mfma16(A, (MASK) ? v2u{v[2], v[3]} : v2u{0u, 0u});                                              \
    }                                                                                                                \
    _Pragma("unroll") for (int r = 0; r < 4; ++r)                                                                    \
      __builtin_amdgcn_raw_buffer_store_b128(v4u{cvt_pk(o[0][r], o[1][r]), cvt_pk(o[2][r], o[3][r]), cvt_pk(o[4][r], o[5][r]), cvt_pk(o[6][r], o[7][r])}, \
                                             ro, ob + r * 768 + (OFFS), 0, AUX_OUT);                                  \
  }
  PV_GROUP(pe, vb[0], true, 0)
  PV_GROUP(po, vb[1], true, 512)
  const bool mine = (n < 8) == (lane < 32);
  PV_GROUP(pa, vb[2], mine, 256)
}

extern "C" void kernel_launch(void* const* d_in, const int* in_sizes, int n_in, void* d_out, int out_size, void* d_ws, size_t ws_size,
                              hipStream_t stream) {
  (void)in_sizes; (void)n_in; (void)out_size; (void)ws_size;
  (void)hipFuncSetAttribute((const void*)linformer_fused, hipFuncAttributeMaxDynamicSharedMemorySize, LDS_BYTES);
  linformer_fused<<<NWG, NTHR, LDS_BYTES, stream>>>((const uint16_t*)d_in[0], (const uint16_t*)d_in[1], (const uint16_t*)d_in[2],
                                                    (const uint16_t*)d_in[3], (const uint16_t*)d_in[4], (uint16_t*)d_out, (unsigned char*)d_ws);
}
```
